```python
import torch

def _softmax(x, axis=-1):
    return torch.softmax(x, dim=axis)
def _gelu(x):
    return torch.nn.functional.gelu(x, approximate="tanh")
def _silu(x):
    return torch.nn.functional.silu(x)
def _sigmoid(x):
    return torch.sigmoid(x)
def _layernorm(x, axis=-1, eps=1e-5):
    m = x.mean(dim=axis, keepdim=True)
    v = x.var(dim=axis, keepdim=True, unbiased=False)
    return (x - m) / torch.sqrt(v + eps)
def _scatter_add(tgt, idx, src, axis=0):
    return tgt.clone().index_add_(axis, idx, src)
def _segment_sum(src, seg, n):
    out = torch.zeros((n,) + src.shape[1:], dtype=src.dtype)
    return out.index_add_(0, seg, src)
def _pad(x, pw):
    flat = tuple(v for pair in reversed(pw) for v in pair)
    return torch.nn.functional.pad(x, flat)

kernel_name = 'standalone__se_block__vision'

def setup_inputs():

    _w = (lambda a: (lambda t: t.to(torch.bfloat16) if t.is_floating_point() else t)(torch.as_tensor(a)))
    H = W = 512
    C = 192; r = max(C // 16, 4)
    return {
        "x": _w((lambda *s: torch.randn(*s, dtype=torch.bfloat16) * 3.2)(1, H, W, C)),
        "w_down": _w((lambda *s: torch.randn(*s, dtype=torch.bfloat16) * 3.2)(C, r)),
        "w_up": _w((lambda *s: torch.randn(*s, dtype=torch.bfloat16) * 3.2)(r, C)),
    }

def reference(x, w_down, w_up):
    s = x.mean(axis=(1, 2))
    s = _sigmoid(_silu(s @ w_down) @ w_up)
    return x * s[:, None, None, :]
```

```cpp
#include <hip/hip_runtime.h>
#include <hip/hip_bf16.h>
#include <stdint.h>

typedef unsigned int u32;
typedef u32 u32x4 __attribute__((ext_vector_type(4)));

#define C 192
#define R 12
#define NB1 512
#define T1 768

__device__ __forceinline__ float bf_lo(u32 v) { return __uint_as_float(v << 16); }
__device__ __forceinline__ float bf_hi(u32 v) { return __uint_as_float(v & 0xffff0000u); }
__device__ __forceinline__ float bfround(float f) { return __bfloat162float(__float2bfloat16(f)); }
__device__ __forceinline__ u32 pack_bf16(float a, float b) {
    typedef __bf16 bf16x2 __attribute__((ext_vector_type(2)));
    typedef float f32x2 __attribute__((ext_vector_type(2)));
    f32x2 v = {a, b};
    bf16x2 r = __builtin_convertvector(v, bf16x2);
    return __builtin_bit_cast(u32, r);
}

__global__ void __launch_bounds__(T1) k_sum(const u32x4* __restrict__ x, float* __restrict__ part, int nchunk) {
    __shared__ float red[T1 * 8];
    const int t = threadIdx.x;
    float acc[8];
#pragma unroll
    for (int j = 0; j < 8; ++j) acc[j] = 0.f;
    const int per = nchunk / NB1;
    const u32x4* p = x + (size_t)blockIdx.x * per + t;
#pragma unroll 8
    for (int i = 0; i < per / T1; ++i) {
        u32x4 v = p[i * T1];
#pragma unroll
        for (int k = 0; k < 4; ++k) { acc[2 * k] += bf_lo(v[k]); acc[2 * k + 1] += bf_hi(v[k]); }
    }
#pragma unroll
    for (int j = 0; j < 8; ++j) red[t * 8 + j] = acc[j];
    __syncthreads();
    if (t < C) {
        float s = 0.f;
        for (int r = 0; r < T1 / 24; ++r) s += red[r * C + t];
        part[blockIdx.x * C + t] = s;
    }
}

__global__ void __launch_bounds__(T1) k_scale(const u32x4* __restrict__ x, const __hip_bfloat16* __restrict__ wd,
                                              const __hip_bfloat16* __restrict__ wu, const float* __restrict__ part,
                                              u32x4* __restrict__ out, int nchunk, float inv_n) {
    __shared__ float red[4 * C];
    __shared__ float mean[C];
    __shared__ float h[R];
    __shared__ float sv[C];
    const int t = threadIdx.x;
    {
        int c = t % C, q = t / C;
        float s = 0.f;
        for (int b = q; b < NB1; b += 4) s += part[b * C + c];
        red[q * C + c] = s;
    }
    __syncthreads();
    if (t < C) mean[t] = bfround((red[t] + red[C + t] + red[2 * C + t] + red[3 * C + t]) * inv_n);
    __syncthreads();
    if (t < R) {
        float z = 0.f;
        for (int c = 0; c < C; ++c) z += mean[c] * __bfloat162float(wd[c * R + t]);
        z = bfround(z);
        h[t] = bfround(z / (1.f + __expf(-z)));
    }
    __syncthreads();
    if (t < C) {
        float u = 0.f;
        for (int j = 0; j < R; ++j) u += h[j] * __bfloat162float(wu[j * C + t]);
        u = bfround(u);
        sv[t] = bfround(1.f / (1.f + __expf(-u)));
    }
    __syncthreads();
    float s[8];
#pragma unroll
    for (int j = 0; j < 8; ++j) s[j] = sv[(t % 24) * 8 + j];
    const int per = nchunk / gridDim.x;
    const u32x4* p = x + (size_t)blockIdx.x * per + t;
    u32x4* o = out + (size_t)blockIdx.x * per + t;
#pragma unroll 8
    for (int i = 0; i < per / T1; ++i) {
        u32x4 v = p[i * T1];
        u32x4 r;
#pragma unroll
        for (int k = 0; k < 4; ++k) r[k] = pack_bf16(bf_lo(v[k]) * s[2 * k], bf_hi(v[k]) * s[2 * k + 1]);
        o[i * T1] = r;
    }
}

extern "C" void kernel_launch(void* const* d_in, const int* in_sizes, int n_in,
                              void* d_out, int out_size, void* d_ws, size_t ws_size,
                              hipStream_t stream) {
    const u32x4* x = (const u32x4*)d_in[0];
    const __hip_bfloat16* wd = (const __hip_bfloat16*)d_in[1];
    const __hip_bfloat16* wu = (const __hip_bfloat16*)d_in[2];
    int n = in_sizes[0];
    int nchunk = n / 8;
    float* part = (float*)d_ws;
    k_sum<<<NB1, T1, 0, stream>>>(x, part, nchunk);
    k_scale<<<512, T1, 0, stream>>>(x, wd, wu, part, (u32x4*)d_out, nchunk, 1.0f / (float)(n / C));
}
```

```cpp
#include <hip/hip_runtime.h>
#include <hip/hip_bf16.h>
#include <stdint.h>

typedef unsigned int u32;
typedef unsigned long long u64;
typedef u32 u32x4 __attribute__((ext_vector_type(4)));
typedef float f32x2 __attribute__((ext_vector_type(2)));
typedef float f32x4 __attribute__((ext_vector_type(4)));
typedef __attribute__((address_space(1))) u32 gu32;
typedef __attribute__((address_space(1))) u64 gu64;

#define C 192
#define R 12
#define NWG 256
#define NT 768
#define NL 32
#define LD_POL " nt"
#define ST_POL ""
#define SPIN_CAP (1u << 20)

#define CL10(p) "v" #p "0","v" #p "1","v" #p "2","v" #p "3","v" #p "4","v" #p "5","v" #p "6","v" #p "7","v" #p "8","v" #p "9"
#define DATA_REGS CL10(4), CL10(5), CL10(6), CL10(7), CL10(8), CL10(9), CL10(10), CL10(11), CL10(12), CL10(13), CL10(14), CL10(15), \
    "v160","v161","v162","v163","v164","v165","v166","v167"

__device__ __forceinline__ float bf_lo(u32 v) { return __uint_as_float(v << 16); }
__device__ __forceinline__ u32 pack_bf16(float a, float b) {
    typedef __bf16 bf16x2 __attribute__((ext_vector_type(2)));
    f32x2 v = {a, b};
    bf16x2 r = __builtin_convertvector(v, bf16x2);
    return __builtin_bit_cast(u32, r);
}
__device__ __forceinline__ float bfround(float f) { return bf_lo(pack_bf16(f, f)); }
__device__ __forceinline__ float bf2f(unsigned short h) { return __uint_as_float((u32)h << 16); }

template <int CTRL, int ROWMASK>
__device__ __forceinline__ float dpp_add(float v) {
    return v + __int_as_float(__builtin_amdgcn_update_dpp(0, __float_as_int(v), CTRL, ROWMASK, 0xf, true));
}
__device__ __forceinline__ float wave_sum(float v) {
    v = dpp_add<0x111, 0xf>(v);
    v = dpp_add<0x112, 0xf>(v);
    v = dpp_add<0x114, 0xf>(v);
    v = dpp_add<0x118, 0xf>(v);
    v = dpp_add<0x142, 0xa>(v);
    v = dpp_add<0x143, 0xc>(v);
    return __int_as_float(__builtin_amdgcn_readlane(__float_as_int(v), 63));
}

#define STAMP_AT(k) do { } while (0)

__global__ void __launch_bounds__(NT, 3) __attribute__((amdgpu_num_vgpr(20)))
se_kernel(const u32x4* __restrict__ x, const u32x4* __restrict__ wd, const u32x4* __restrict__ wu,
          u32x4* __restrict__ out, u32* __restrict__ ws) {
    __shared__ __attribute__((aligned(16))) float lds[32 * C + 4 * C + C + 16 + 4 + 2 * 1152];
    float* red = lds;
    float* red2 = lds + 32 * C;
    float* sv = red2 + 4 * C;
    float* hv = sv + C;
    u32* tagw = (u32*)(hv + 16);
    u32x4* wl = (u32x4*)(hv + 20);
    const unsigned short* wdl = (const unsigned short*)wl;
    const unsigned short* wul = wdl + C * R;

    const int t = threadIdx.x;
    const int lane = t & 63;
    const int w = __builtin_amdgcn_readfirstlane(t >> 6);
    const int b = blockIdx.x;
    STAMP_AT(0);

    gu32* epoch = (gu32*)ws;
    gu64* gran = (gu64*)(ws + 64);

    const auto rx = __builtin_amdgcn_make_buffer_rsrc((void*)(x + (size_t)b * (NT * NL)), 0, NT * NL * 16, 0x00020000);
    const auto ro = __builtin_amdgcn_make_buffer_rsrc((void*)(out + (size_t)b * (NT * NL)), 0, NT * NL * 16, 0x00020000);
    const int voff = t * 16;
    const int wi = t < 576 ? t : 575;
    const u32x4* wp = wi < 288 ? wd + wi : wu + (wi - 288);

    float a0, a1, a2, a3, a4, a5, a6, a7;
    u32x4 wv;
    u32 e0;
    {
        float t0, t1, t2, t3;
        u32 so;
        asm volatile(
            "s_cmp_lg_u32 %[w], 0\n\t"
            "s_cbranch_scc1 1f\n\t"
            "v_mov_b32 %[e], 0\n\t"
            "global_load_dword %[e], %[e], %[ep] sc1\n"
            "1:\n\t"
            ".set se_i, 0\n\t"
            ".rept 32\n\t"
            "s_mov_b32 %[so], se_i*12288\n\t"
            "buffer_load_dwordx4 v[40+4*se_i:43+4*se_i], %[voff], %[rx], %[so] offen" LD_POL "\n\t"
            ".set se_i, se_i+1\n\t"
            ".endr\n\t"
            "global_load_dwordx4 %[wv], %[wp], off\n\t"
            "v_mov_b32 %[a0], 0\n\tv_mov_b32 %[a1], 0\n\tv_mov_b32 %[a2], 0\n\tv_mov_b32 %[a3], 0\n\t"
            "v_mov_b32 %[a4], 0\n\tv_mov_b32 %[a5], 0\n\tv_mov_b32 %[a6], 0\n\tv_mov_b32 %[a7], 0\n\t"
            ".set se_i, 0\n\t"
            ".rept 32\n\t"
            "s_waitcnt vmcnt(32-se_i)\n\t"
            "v_lshlrev_b32 %[t0], 16, v[40+4*se_i]\n\t"
            "v_and_b32 %[t1], %[mask], v[40+4*se_i]\n\t"
            "v_lshlrev_b32 %[t2], 16, v[41+4*se_i]\n\t"
            "v_and_b32 %[t3], %[mask], v[41+4*se_i]\n\t"
            "v_add_f32 %[a0], %[a0], %[t0]\n\t"
            "v_add_f32 %[a1], %[a1], %[t1]\n\t"
            "v_add_f32 %[a2], %[a2], %[t2]\n\t"
            "v_add_f32 %[a3], %[a3], %[t3]\n\t"
            "v_lshlrev_b32 %[t0], 16, v[42+4*se_i]\n\t"
            "v_and_b32 %[t1], %[mask], v[42+4*se_i]\n\t"
            "v_lshlrev_b32 %[t2], 16, v[43+4*se_i]\n\t"
            "v_and_b32 %[t3], %[mask], v[43+4*se_i]\n\t"
            "v_add_f32 %[a4], %[a4], %[t0]\n\t"
            "v_add_f32 %[a5], %[a5], %[t1]\n\t"
            "v_add_f32 %[a6], %[a6], %[t2]\n\t"
            "v_add_f32 %[a7], %[a7], %[t3]\n\t"
            ".set se_i, se_i+1\n\t"
            ".endr\n\t"
            "s_waitcnt vmcnt(0)\n\t"
            : [a0] "=&v"(a0), [a1] "=&v"(a1), [a2] "=&v"(a2), [a3] "=&v"(a3),
              [a4] "=&v"(a4), [a5] "=&v"(a5), [a6] "=&v"(a6), [a7] "=&v"(a7),
              [t0] "=&v"(t0), [t1] "=&v"(t1), [t2] "=&v"(t2), [t3] "=&v"(t3),
              [wv] "=&v"(wv), [e] "=&v"(e0), [so] "=&s"(so)
            : [w] "s"(w), [ep] "s"(epoch), [voff] "v"(voff), [rx] "s"(rx), [wp] "v"(wp), [mask] "s"(0xffff0000u)
            : DATA_REGS, "scc", "memory");
    }
    STAMP_AT(1);
    if (t == 0) *tagw = e0 + 1;
    if (t < 576) wl[t] = wv;
    *(f32x4*)(red + t * 8) = f32x4{a0, a1, a2, a3};
    *(f32x4*)(red + t * 8 + 4) = f32x4{a4, a5, a6, a7};
    __syncthreads();
    {
        const int q = t / C, c = t % C;
        float s = 0.f;
#pragma unroll
        for (int r = 0; r < 8; ++r) s += red[(q * 8 + r) * C + c];
        red2[q * C + c] = s;
    }
    float wdv[3];
#pragma unroll
    for (int k = 0; k < 3; ++k) wdv[k] = bf2f(wdl[(3 * lane + k) * R + w]);
    __syncthreads();
    const u32 tag = *tagw;
    float zp = 0.f;
#pragma unroll
    for (int k = 0; k < 3; ++k) {
        const int c = 3 * lane + k;
        zp += ((red2[c] + red2[C + c]) + (red2[2 * C + c] + red2[3 * C + c])) * wdv[k];
    }
    zp = wave_sum(zp);
    if (lane == 0)
        __hip_atomic_store(gran + w * NWG + b, ((u64)tag << 32) | __float_as_uint(zp), __ATOMIC_RELAXED, __HIP_MEMORY_SCOPE_AGENT);
    STAMP_AT(2);

    float wuv[R];
    if (t < C) {
#pragma unroll
        for (int j = 0; j < R; ++j) wuv[j] = bf2f(wul[j * C + t]);
    }

    float z;
    {
        gu64* g = gran + w * NWG + lane;
        for (u32 spin = 0; spin < SPIN_CAP; ++spin) {
            u64 q0 = __hip_atomic_load(g, __ATOMIC_RELAXED, __HIP_MEMORY_SCOPE_AGENT);
            u64 q1 = __hip_atomic_load(g + 64, __ATOMIC_RELAXED, __HIP_MEMORY_SCOPE_AGENT);
            u64 q2 = __hip_atomic_load(g + 128, __ATOMIC_RELAXED, __HIP_MEMORY_SCOPE_AGENT);
            u64 q3 = __hip_atomic_load(g + 192, __ATOMIC_RELAXED, __HIP_MEMORY_SCOPE_AGENT);
            bool ok = ((u32)(q0 >> 32) == tag) & ((u32)(q1 >> 32) == tag) & ((u32)(q2 >> 32) == tag) & ((u32)(q3 >> 32) == tag);
            z = (__uint_as_float((u32)q0) + __uint_as_float((u32)q1)) + (__uint_as_float((u32)q2) + __uint_as_float((u32)q3));
            if (__all(ok)) break;
            __builtin_amdgcn_s_sleep(1);
        }
    }
    STAMP_AT(3);
    if (b == 0 && t == 0) __hip_atomic_store(epoch, tag, __ATOMIC_RELAXED, __HIP_MEMORY_SCOPE_AGENT);
    z = wave_sum(z) * (1.0f / (512.f * 512.f));
    z = bfround(z);
    if (lane == 0) hv[w] = bfround(z / (1.f + __expf(-z)));
    __syncthreads();
    if (t < C) {
        float u = 0.f;
#pragma unroll
        for (int j = 0; j < R; ++j) u += hv[j] * wuv[j];
        u = bfround(u);
        sv[t] = bfround(1.f / (1.f + __expf(-u)));
    }
    __syncthreads();
    {
        const int g = t % 24;
        f32x4 sa = *(const f32x4*)(sv + g * 8), sb = *(const f32x4*)(sv + g * 8 + 4);
        float t0, t1, t2, t3;
        u32 so;
        STAMP_AT(4);
        asm volatile(
            "s_nop 1\n\t"
            ".set se_i, 0\n\t"
            ".rept 32\n\t"
            "v_lshlrev_b32 %[t0], 16, v[40+4*se_i]\n\t"
            "v_and_b32 %[t1], %[mask], v[40+4*se_i]\n\t"
            "v_lshlrev_b32 %[t2], 16, v[41+4*se_i]\n\t"
            "v_and_b32 %[t3], %[mask], v[41+4*se_i]\n\t"
            "v_mul_f32 %[t0], %[t0], %[s0]\n\t"
            "v_mul_f32 %[t1], %[t1], %[s1]\n\t"
            "v_mul_f32 %[t2], %[t2], %[s2]\n\t"
            "v_mul_f32 %[t3], %[t3], %[s3]\n\t"
            "v_cvt_pk_bf16_f32 v[40+4*se_i], %[t0], %[t1]\n\t"
            "v_cvt_pk_bf16_f32 v[41+4*se_i], %[t2], %[t3]\n\t"
            "v_lshlrev_b32 %[t0], 16, v[42+4*se_i]\n\t"
            "v_and_b32 %[t1], %[mask], v[42+4*se_i]\n\t"
            "v_lshlrev_b32 %[t2], 16, v[43+4*se_i]\n\t"
            "v_and_b32 %[t3], %[mask], v[43+4*se_i]\n\t"
            "v_mul_f32 %[t0], %[t0], %[s4]\n\t"
            "v_mul_f32 %[t1], %[t1], %[s5]\n\t"
            "v_mul_f32 %[t2], %[t2], %[s6]\n\t"
            "v_mul_f32 %[t3], %[t3], %[s7]\n\t"
            "s_mov_b32 %[so], se_i*12288\n\t"
            "v_cvt_pk_bf16_f32 v[42+4*se_i], %[t0], %[t1]\n\t"
            "v_cvt_pk_bf16_f32 v[43+4*se_i], %[t2], %[t3]\n\t"
            "buffer_store_dwordx4 v[40+4*se_i:43+4*se_i], %[voff], %[ro], %[so] offen" ST_POL "\n\t"
            ".set se_i, se_i+1\n\t"
            ".endr\n\t"
            : [t0] "=&v"(t0), [t1] "=&v"(t1), [t2] "=&v"(t2), [t3] "=&v"(t3), [so] "=&s"(so)
            : [s0] "v"(sa[0]), [s1] "v"(sa[1]), [s2] "v"(sa[2]), [s3] "v"(sa[3]),
              [s4] "v"(sb[0]), [s5] "v"(sb[1]), [s6] "v"(sb[2]), [s7] "v"(sb[3]),
              [voff] "v"(voff), [ro] "s"(ro), [mask] "s"(0xffff0000u)
            : DATA_REGS, "memory");
    }
}

extern "C" void kernel_launch(void* const* d_in, const int* in_sizes, int n_in,
                              void* d_out, int out_size, void* d_ws, size_t ws_size,
                              hipStream_t stream) {
    se_kernel<<<NWG, NT, 0, stream>>>((const u32x4*)d_in[0], (const u32x4*)d_in[1], (const u32x4*)d_in[2],
                                      (u32x4*)d_out, (u32*)d_ws);
}
```

```cpp
#include <hip/hip_runtime.h>
#include <hip/hip_bf16.h>
#include <stdint.h>

typedef unsigned int u32;
typedef unsigned long long u64;
typedef u32 u32x4 __attribute__((ext_vector_type(4)));
typedef u32 u32x2 __attribute__((ext_vector_type(2)));
typedef float f32x2 __attribute__((ext_vector_type(2)));
typedef float f32x4 __attribute__((ext_vector_type(4)));
typedef __attribute__((address_space(1))) u32 gu32;
typedef __attribute__((address_space(1))) u64 gu64;

#define C 192
#define R 12
#define NWG 256
#define NT 768
#define NL 32
#define LD_POL " nt"
#define ST_POL " sc0 sc1"
#define SPIN_CAP (1u << 20)
#define LEADER 0
#define NLEAD 8
#define PRE_SLEEP 4
#define POLL_SLEEP 0
#define HSLEEP 1

#define CL10(p) "v" #p "0","v" #p "1","v" #p "2","v" #p "3","v" #p "4","v" #p "5","v" #p "6","v" #p "7","v" #p "8","v" #p "9"
#define DATA_REGS CL10(4), CL10(5), CL10(6), CL10(7), CL10(8), CL10(9), CL10(10), CL10(11), CL10(12), CL10(13), CL10(14), CL10(15), \
    "v160","v161","v162","v163","v164","v165","v166","v167"

__device__ __forceinline__ float bf_lo(u32 v) { return __uint_as_float(v << 16); }
__device__ __forceinline__ u32 pack_bf16(float a, float b) {
    typedef __bf16 bf16x2 __attribute__((ext_vector_type(2)));
    f32x2 v = {a, b};
    bf16x2 r = __builtin_convertvector(v, bf16x2);
    return __builtin_bit_cast(u32, r);
}
__device__ __forceinline__ float bfround(float f) { return bf_lo(pack_bf16(f, f)); }
__device__ __forceinline__ float bf2f(unsigned short h) { return __uint_as_float((u32)h << 16); }

template <int CTRL>
__device__ __forceinline__ float dpp_add(float v) {
    return v + __int_as_float(__builtin_amdgcn_update_dpp(0, __float_as_int(v), CTRL, 0xf, 0xf, true));
}
__device__ __forceinline__ float add_xor16(float v) {
    auto r = __builtin_amdgcn_permlane16_swap(__float_as_uint(v), __float_as_uint(v), false, false);
    return __uint_as_float(r[0]) + __uint_as_float(r[1]);
}
__device__ __forceinline__ float add_xor32(float v) {
    auto r = __builtin_amdgcn_permlane32_swap(__float_as_uint(v), __float_as_uint(v), false, false);
    return __uint_as_float(r[0]) + __uint_as_float(r[1]);
}

#define STAMP_AT(k) do { } while (0)

#define RS 112
#define PL (32 * RS)
#define L_RED 0
#define L_PART (2 * PL)
#define L_PART2 (L_PART + 256)
#define L_SV (L_PART2 + 128)
#define L_TAG (L_SV + C)
#define L_W (L_TAG + 4)
#define L_END (L_W + 2 * 1152)

__global__ void __launch_bounds__(NT, 3) __attribute__((amdgpu_num_vgpr(20)))
se_kernel(const u32x4* __restrict__ x, const u32x4* __restrict__ wd, const u32x4* __restrict__ wu,
          u32x4* __restrict__ out, u32* __restrict__ ws) {
    __shared__ __attribute__((aligned(16))) float lds[L_END];
    u32x4* wl = (u32x4*)(lds + L_W);
    const unsigned short* wdl = (const unsigned short*)wl;
    const unsigned short* wul = wdl + C * R;

    const int t = threadIdx.x;
    const int lane = t & 63;
    const int w = __builtin_amdgcn_readfirstlane(t >> 6);
    const int b = blockIdx.x;
    STAMP_AT(0);

    gu32* epoch = (gu32*)ws;
    gu64* hgran = (gu64*)(ws + 64);
    gu64* gran = (gu64*)(ws + 1024);

    const auto rx = __builtin_amdgcn_make_buffer_rsrc((void*)(x + (size_t)b * (NT * NL)), 0, NT * NL * 16, 0x00020000);
    const auto ro = __builtin_amdgcn_make_buffer_rsrc((void*)(out + (size_t)b * (NT * NL)), 0, NT * NL * 16, 0x00020000);
    const int voff = t * 16;
    const int wi = t < 576 ? t : 575;
    const u32x4* wp = wi < 288 ? wd + wi : wu + (wi - 288);

    float a0, a1, a2, a3, a4, a5, a6, a7;
    u32x4 wv;
    u32 e0;
    {
        float t0, t1, t2, t3;
        u32 so;
        asm volatile(
            "s_cmp_lg_u32 %[w], 0\n\t"
            "s_cbranch_scc1 1f\n\t"
            "v_mov_b32 %[e], 0\n\t"
            "global_load_dword %[e], %[e], %[ep] sc1\n"
            "1:\n\t"
            ".set se_i, 0\n\t"
            ".rept 32\n\t"
            "s_mov_b32 %[so], se_i*12288\n\t"
            "buffer_load_dwordx4 v[40+4*se_i:43+4*se_i], %[voff], %[rx], %[so] offen" LD_POL "\n\t"
            ".set se_i, se_i+1\n\t"
            ".endr\n\t"
            "global_load_dwordx4 %[wv], %[wp], off\n\t"
            "v_mov_b32 %[a0], 0\n\tv_mov_b32 %[a1], 0\n\tv_mov_b32 %[a2], 0\n\tv_mov_b32 %[a3], 0\n\t"
            "v_mov_b32 %[a4], 0\n\tv_mov_b32 %[a5], 0\n\tv_mov_b32 %[a6], 0\n\tv_mov_b32 %[a7], 0\n\t"
            ".set se_i, 0\n\t"
            ".rept 32\n\t"
            "s_waitcnt vmcnt(32-se_i)\n\t"
            "v_lshlrev_b32 %[t0], 16, v[40+4*se_i]\n\t"
            "v_and_b32 %[t1], %[mask], v[40+4*se_i]\n\t"
            "v_lshlrev_b32 %[t2], 16, v[41+4*se_i]\n\t"
            "v_and_b32 %[t3], %[mask], v[41+4*se_i]\n\t"
            "v_add_f32 %[a0], %[a0], %[t0]\n\t"
            "v_add_f32 %[a1], %[a1], %[t1]\n\t"
            "v_add_f32 %[a2], %[a2], %[t2]\n\t"
            "v_add_f32 %[a3], %[a3], %[t3]\n\t"
            "v_lshlrev_b32 %[t0], 16, v[42+4*se_i]\n\t"
            "v_and_b32 %[t1], %[mask], v[42+4*se_i]\n\t"
            "v_lshlrev_b32 %[t2], 16, v[43+4*se_i]\n\t"
            "v_and_b32 %[t3], %[mask], v[43+4*se_i]\n\t"
            "v_add_f32 %[a4], %[a4], %[t0]\n\t"
            "v_add_f32 %[a5], %[a5], %[t1]\n\t"
            "v_add_f32 %[a6], %[a6], %[t2]\n\t"
            "v_add_f32 %[a7], %[a7], %[t3]\n\t"
            ".set se_i, se_i+1\n\t"
            ".endr\n\t"
            "s_waitcnt vmcnt(0)\n\t"
            : [a0] "=&v"(a0), [a1] "=&v"(a1), [a2] "=&v"(a2), [a3] "=&v"(a3),
              [a4] "=&v"(a4), [a5] "=&v"(a5), [a6] "=&v"(a6), [a7] "=&v"(a7),
              [t0] "=&v"(t0), [t1] "=&v"(t1), [t2] "=&v"(t2), [t3] "=&v"(t3),
              [wv] "=&v"(wv), [e] "=&v"(e0), [so] "=&s"(so)
            : [w] "s"(w), [ep] "s"(epoch), [voff] "v"(voff), [rx] "s"(rx), [wp] "v"(wp), [mask] "s"(0xffff0000u)
            : DATA_REGS, "scc", "memory");
    }
    STAMP_AT(1);
    if (t == 0) ((u32*)lds)[L_TAG] = e0 + 1;
    if (t < 576) wl[t] = wv;
    {
        const int r = t / 24, g = t - r * 24;
        *(f32x4*)(lds + L_RED + r * RS + g * 4) = f32x4{a0, a1, a2, a3};
        *(f32x4*)(lds + L_RED + PL + r * RS + g * 4) = f32x4{a4, a5, a6, a7};
    }
    __syncthreads();
    const int q = lane >> 4;
    {
        const int s = 16 * w + (lane & 15);
        const int pl = s >= 96 ? 1 : 0, idx = s - 96 * pl;
        const int c = 8 * (idx >> 2) + 4 * pl + (idx & 3);
        const float* src = lds + L_RED + pl * PL + q * RS + idx;
        float v0 = src[0], v1 = src[4 * RS], v2 = src[8 * RS], v3 = src[12 * RS];
        float v4 = src[16 * RS], v5 = src[20 * RS], v6 = src[24 * RS], v7 = src[28 * RS];
        const unsigned short* wq = wdl + c * R + 3 * q;
        const float w0 = bf2f(wq[0]), w1 = bf2f(wq[1]), w2 = bf2f(wq[2]);
        float sc = ((v0 + v1) + (v2 + v3)) + ((v4 + v5) + (v6 + v7));
        sc = add_xor32(add_xor16(sc));
        float p0 = sc * w0, p1 = sc * w1, p2 = sc * w2;
        p0 = dpp_add<0x111>(p0); p1 = dpp_add<0x111>(p1); p2 = dpp_add<0x111>(p2);
        p0 = dpp_add<0x112>(p0); p1 = dpp_add<0x112>(p1); p2 = dpp_add<0x112>(p2);
        p0 = dpp_add<0x114>(p0); p1 = dpp_add<0x114>(p1); p2 = dpp_add<0x114>(p2);
        p0 = dpp_add<0x118>(p0); p1 = dpp_add<0x118>(p1); p2 = dpp_add<0x118>(p2);
        if ((lane & 15) == 15) {
            float* dst = lds + L_PART + (3 * q) * 16 + w;
            dst[0] = p0; dst[16] = p1; dst[32] = p2;
        }
    }
    __syncthreads();
    const u32 tag = ((const u32*)lds)[L_TAG];
    if (w == 0) {
        f32x4 pv = *(const f32x4*)(lds + L_PART + lane * 4);
        float z = (lane & 3) == 3 ? 0.f : (pv[0] + pv[1]) + (pv[2] + pv[3]);
        z = dpp_add<0xb1>(z);
        z = dpp_add<0x4e>(z);
        if ((lane & 3) == 0)
            __hip_atomic_store(gran + b * 16 + (lane >> 2), ((u64)tag << 32) | __float_as_uint(z), __ATOMIC_RELAXED, __HIP_MEMORY_SCOPE_AGENT);
    }
    STAMP_AT(2);
    float wuv[R];
    if (w < 3) {
#pragma unroll
        for (int j = 0; j < R; ++j) wuv[j] = bf2f(wul[j * C + t]);
    }
    if (w < 8) {
        const auto rg = __builtin_amdgcn_make_buffer_rsrc((void*)(ws + 1024), 0, NWG * 128, 0x00020000);
        const int off = (32 * w + (lane >> 3)) * 128 + (lane & 7) * 16;
        u32x4 g0 = {0, 0, 0, 0}, g1 = g0, g2 = g0, g3 = g0;
        u64 m0 = 0x3f3f3f3f3f3f3f3full, m1 = m0, m2 = m0, m3 = m0;
        if (PRE_SLEEP) __builtin_amdgcn_s_sleep(PRE_SLEEP);
        for (u32 spin = 0; spin < SPIN_CAP; ++spin) {
            asm volatile(
                "s_mov_b64 exec, %[m0]\n\t"
                "s_cbranch_execz 2f\n\t"
                "buffer_load_dwordx4 %[g0], %[off], %[rg], 0 offen sc1\n"
                "2:\n\t"
                "s_mov_b64 exec, %[m1]\n\t"
                "s_cbranch_execz 3f\n\t"
                "buffer_load_dwordx4 %[g1], %[off], %[rg], 0 offen offset:1024 sc1\n"
                "3:\n\t"
                "s_mov_b64 exec, %[m2]\n\t"
                "s_cbranch_execz 4f\n\t"
                "buffer_load_dwordx4 %[g2], %[off], %[rg], 0 offen offset:2048 sc1\n"
                "4:\n\t"
                "s_mov_b64 exec, %[m3]\n\t"
                "s_cbranch_execz 5f\n\t"
                "buffer_load_dwordx4 %[g3], %[off], %[rg], 0 offen offset:3072 sc1\n"
                "5:\n\t"
                "s_mov_b64 exec, -1\n\t"
                "s_waitcnt vmcnt(0)"
                : [g0] "+v"(g0), [g1] "+v"(g1), [g2] "+v"(g2), [g3] "+v"(g3)
                : [m0] "s"(m0), [m1] "s"(m1), [m2] "s"(m2), [m3] "s"(m3), [off] "v"(off), [rg] "s"(rg)
                : "memory");
            m0 &= __ballot((g0[1] != tag) | (g0[3] != tag));
            m1 &= __ballot((g1[1] != tag) | (g1[3] != tag));
            m2 &= __ballot((g2[1] != tag) | (g2[3] != tag));
            m3 &= __ballot((g3[1] != tag) | (g3[3] != tag));
            if ((m0 | m1 | m2 | m3) == 0) break;
            if (POLL_SLEEP) __builtin_amdgcn_s_sleep(POLL_SLEEP);
        }
        float z0 = (__uint_as_float(g0[0]) + __uint_as_float(g1[0])) + (__uint_as_float(g2[0]) + __uint_as_float(g3[0]));
        float z1 = (__uint_as_float(g0[2]) + __uint_as_float(g1[2])) + (__uint_as_float(g2[2]) + __uint_as_float(g3[2]));
        z0 = dpp_add<0x128>(z0); z1 = dpp_add<0x128>(z1);
        z0 = add_xor32(add_xor16(z0)); z1 = add_xor32(add_xor16(z1));
        if (lane < 6) { lds[L_PART2 + (2 * lane) * 8 + w] = z0; lds[L_PART2 + (2 * lane + 1) * 8 + w] = z1; }
    }
    __syncthreads();
    STAMP_AT(3);
    if (b == 0 && t == 0) __hip_atomic_store(epoch, tag, __ATOMIC_RELAXED, __HIP_MEMORY_SCOPE_AGENT);
    if (w < 3) {
        f32x4 pv = *(const f32x4*)(lds + L_PART2 + (lane & 31) * 4);
        float z = (pv[0] + pv[1]) + (pv[2] + pv[3]);
        z = dpp_add<0xb1>(z);
        z = bfround(z * (1.0f / (512.f * 512.f)));
        const int hb = __float_as_int(bfround(z * __builtin_amdgcn_rcpf(1.f + __expf(-z))));
        float u = 0.f;
#pragma unroll
        for (int j = 0; j < R; ++j) u += __int_as_float(__builtin_amdgcn_readlane(hb, 2 * j)) * wuv[j];
        u = bfround(u);
        lds[L_SV + t] = bfround(__builtin_amdgcn_rcpf(1.f + __expf(-u)));
    }
    __syncthreads();
    {
        const int g = t % 24;
        f32x4 sa = *(const f32x4*)(lds + L_SV + g * 8), sb = *(const f32x4*)(lds + L_SV + g * 8 + 4);
        float t0, t1, t2, t3;
        u32 so;
        STAMP_AT(4);
        asm volatile(
            "s_nop 1\n\t"
            ".set se_i, 0\n\t"
            ".rept 32\n\t"
            "v_lshlrev_b32 %[t0], 16, v[40+4*se_i]\n\t"
            "v_and_b32 %[t1], %[mask], v[40+4*se_i]\n\t"
            "v_lshlrev_b32 %[t2], 16, v[41+4*se_i]\n\t"
            "v_and_b32 %[t3], %[mask], v[41+4*se_i]\n\t"
            "v_mul_f32 %[t0], %[t0], %[s0]\n\t"
            "v_mul_f32 %[t1], %[t1], %[s1]\n\t"
            "v_mul_f32 %[t2], %[t2], %[s2]\n\t"
            "v_mul_f32 %[t3], %[t3], %[s3]\n\t"
            "v_cvt_pk_bf16_f32 v[40+4*se_i], %[t0], %[t1]\n\t"
            "v_cvt_pk_bf16_f32 v[41+4*se_i], %[t2], %[t3]\n\t"
            "v_lshlrev_b32 %[t0], 16, v[42+4*se_i]\n\t"
            "v_and_b32 %[t1], %[mask], v[42+4*se_i]\n\t"
            "v_lshlrev_b32 %[t2], 16, v[43+4*se_i]\n\t"
            "v_and_b32 %[t3], %[mask], v[43+4*se_i]\n\t"
            "v_mul_f32 %[t0], %[t0], %[s4]\n\t"
            "v_mul_f32 %[t1], %[t1], %[s5]\n\t"
            "v_mul_f32 %[t2], %[t2], %[s6]\n\t"
            "v_mul_f32 %[t3], %[t3], %[s7]\n\t"
            "s_mov_b32 %[so], se_i*12288\n\t"
            "v_cvt_pk_bf16_f32 v[42+4*se_i], %[t0], %[t1]\n\t"
            "v_cvt_pk_bf16_f32 v[43+4*se_i], %[t2], %[t3]\n\t"
            "buffer_store_dwordx4 v[40+4*se_i:43+4*se_i], %[voff], %[ro], %[so] offen" ST_POL "\n\t"
            ".set se_i, se_i+1\n\t"
            ".endr\n\t"
            : [t0] "=&v"(t0), [t1] "=&v"(t1), [t2] "=&v"(t2), [t3] "=&v"(t3), [so] "=&s"(so)
            : [s0] "v"(sa[0]), [s1] "v"(sa[1]), [s2] "v"(sa[2]), [s3] "v"(sa[3]),
              [s4] "v"(sb[0]), [s5] "v"(sb[1]), [s6] "v"(sb[2]), [s7] "v"(sb[3]),
              [voff] "v"(voff), [ro] "s"(ro), [mask] "s"(0xffff0000u)
            : DATA_REGS, "memory");
    }
}

extern "C" void kernel_launch(void* const* d_in, const int* in_sizes, int n_in,
                              void* d_out, int out_size, void* d_ws, size_t ws_size,
                              hipStream_t stream) {
    se_kernel<<<NWG, NT, 0, stream>>>((const u32x4*)d_in[0], (const u32x4*)d_in[1], (const u32x4*)d_in[2],
                                      (u32x4*)d_out, (u32*)d_ws);
}
```

```cpp
#include <hip/hip_runtime.h>
#include <hip/hip_bf16.h>
#include <stdint.h>

typedef unsigned int u32;
typedef unsigned long long u64;
typedef u32 u32x4 __attribute__((ext_vector_type(4)));
typedef u32 u32x2 __attribute__((ext_vector_type(2)));
typedef float f32x2 __attribute__((ext_vector_type(2)));
typedef float f32x4 __attribute__((ext_vector_type(4)));
typedef __attribute__((address_space(1))) u32 gu32;
typedef __attribute__((address_space(1))) u64 gu64;

#define C 192
#define R 12
#define NWG 256
#define NT 768
#define NL 32
#define LD_POL " nt"
#define ST_POL " sc0 sc1"
#define SPIN_CAP (1u << 20)
#define REMAP 2
#define PACE 1200
#define STR2(x) #x
#define STR(x) STR2(x)
#define PACE_STR STR(PACE)
#define LEADER 0
#define NLEAD 8
#define PRE_SLEEP 4
#define POLL_SLEEP 0
#define HSLEEP 1

#define CL10(p) "v" #p "0","v" #p "1","v" #p "2","v" #p "3","v" #p "4","v" #p "5","v" #p "6","v" #p "7","v" #p "8","v" #p "9"
#define DATA_REGS CL10(4), CL10(5), CL10(6), CL10(7), CL10(8), CL10(9), CL10(10), CL10(11), CL10(12), CL10(13), CL10(14), CL10(15), \
    "v160","v161","v162","v163","v164","v165","v166","v167"

__device__ __forceinline__ float bf_lo(u32 v) { return __uint_as_float(v << 16); }
__device__ __forceinline__ u32 pack_bf16(float a, float b) {
    typedef __bf16 bf16x2 __attribute__((ext_vector_type(2)));
    f32x2 v = {a, b};
    bf16x2 r = __builtin_convertvector(v, bf16x2);
    return __builtin_bit_cast(u32, r);
}
__device__ __forceinline__ float bfround(float f) { return bf_lo(pack_bf16(f, f)); }
__device__ __forceinline__ float bf2f(unsigned short h) { return __uint_as_float((u32)h << 16); }

template <int CTRL>
__device__ __forceinline__ float dpp_add(float v) {
    return v + __int_as_float(__builtin_amdgcn_update_dpp(0, __float_as_int(v), CTRL, 0xf, 0xf, true));
}
__device__ __forceinline__ float add_xor16(float v) {
    auto r = __builtin_amdgcn_permlane16_swap(__float_as_uint(v), __float_as_uint(v), false, false);
    return __uint_as_float(r[0]) + __uint_as_float(r[1]);
}
__device__ __forceinline__ float add_xor32(float v) {
    auto r = __builtin_amdgcn_permlane32_swap(__float_as_uint(v), __float_as_uint(v), false, false);
    return __uint_as_float(r[0]) + __uint_as_float(r[1]);
}

#define STAMP_AT(k) do { } while (0)

#define RS 112
#define PL (32 * RS)
#define L_RED 0
#define L_PART (2 * PL)
#define L_PART2 (L_PART + 256)
#define L_SV (L_PART2 + 128)
#define L_TAG (L_SV + C)
#define L_W (L_TAG + 4)
#define L_END (L_W + 2 * 1152)

__global__ void __launch_bounds__(NT, 3) __attribute__((amdgpu_num_vgpr(20)))
se_kernel(const u32x4* __restrict__ x, const u32x4* __restrict__ wd, const u32x4* __restrict__ wu,
          u32x4* __restrict__ out, u32* __restrict__ ws) {
    __shared__ __attribute__((aligned(16))) float lds[L_END];
    u32x4* wl = (u32x4*)(lds + L_W);
    const unsigned short* wdl = (const unsigned short*)wl;
    const unsigned short* wul = wdl + C * R;

    const int t = threadIdx.x;
    const int lane = t & 63;
    const int w = __builtin_amdgcn_readfirstlane(t >> 6);
    const int b = blockIdx.x;
    STAMP_AT(0);

    gu32* epoch = (gu32*)ws;
    gu64* hgran = (gu64*)(ws + 64);
    gu64* gran = (gu64*)(ws + 1024);

    const int reg = (b & 7) * 32 + (b >> 3);
    const auto rx = __builtin_amdgcn_make_buffer_rsrc((void*)(x + (size_t)reg * (NT * NL)), 0, NT * NL * 16, 0x00020000);
    const auto ro = __builtin_amdgcn_make_buffer_rsrc((void*)(out + (size_t)reg * (NT * NL)), 0, NT * NL * 16, 0x00020000);
    const int voff = t * 16;
    const int wi = t < 576 ? t : 575;
    const u32x4* wp = wi < 288 ? wd + wi : wu + (wi - 288);

    float a0, a1, a2, a3, a4, a5, a6, a7;
    u32x4 wv;
    u32 e0;
    {
        float t0, t1, t2, t3;
        u32 so;
        asm volatile(
            "s_cmp_lg_u32 %[w], 0\n\t"
            "s_cbranch_scc1 1f\n\t"
            "v_mov_b32 %[e], 0\n\t"
            "global_load_dword %[e], %[e], %[ep] sc1\n"
            "1:\n\t"
            ".set se_i, 0\n\t"
            ".rept 32\n\t"
            "s_mov_b32 %[so], se_i*12288\n\t"
            "buffer_load_dwordx4 v[40+4*se_i:43+4*se_i], %[voff], %[rx], %[so] offen" LD_POL "\n\t"
            ".set se_i, se_i+1\n\t"
            ".endr\n\t"
            "global_load_dwordx4 %[wv], %[wp], off\n\t"
            "v_mov_b32 %[a0], 0\n\tv_mov_b32 %[a1], 0\n\tv_mov_b32 %[a2], 0\n\tv_mov_b32 %[a3], 0\n\t"
            "v_mov_b32 %[a4], 0\n\tv_mov_b32 %[a5], 0\n\tv_mov_b32 %[a6], 0\n\tv_mov_b32 %[a7], 0\n\t"
            ".set se_i, 0\n\t"
            ".rept 32\n\t"
            "s_waitcnt vmcnt(32-se_i)\n\t"
            "v_lshlrev_b32 %[t0], 16, v[40+4*se_i]\n\t"
            "v_and_b32 %[t1], %[mask], v[40+4*se_i]\n\t"
            "v_lshlrev_b32 %[t2], 16, v[41+4*se_i]\n\t"
            "v_and_b32 %[t3], %[mask], v[41+4*se_i]\n\t"
            "v_add_f32 %[a0], %[a0], %[t0]\n\t"
            "v_add_f32 %[a1], %[a1], %[t1]\n\t"
            "v_add_f32 %[a2], %[a2], %[t2]\n\t"
            "v_add_f32 %[a3], %[a3], %[t3]\n\t"
            "v_lshlrev_b32 %[t0], 16, v[42+4*se_i]\n\t"
            "v_and_b32 %[t1], %[mask], v[42+4*se_i]\n\t"
            "v_lshlrev_b32 %[t2], 16, v[43+4*se_i]\n\t"
            "v_and_b32 %[t3], %[mask], v[43+4*se_i]\n\t"
            "v_add_f32 %[a4], %[a4], %[t0]\n\t"
            "v_add_f32 %[a5], %[a5], %[t1]\n\t"
            "v_add_f32 %[a6], %[a6], %[t2]\n\t"
            "v_add_f32 %[a7], %[a7], %[t3]\n\t"
            ".set se_i, se_i+1\n\t"
            ".endr\n\t"
            "s_waitcnt vmcnt(0)\n\t"
            : [a0] "=&v"(a0), [a1] "=&v"(a1), [a2] "=&v"(a2), [a3] "=&v"(a3),
              [a4] "=&v"(a4), [a5] "=&v"(a5), [a6] "=&v"(a6), [a7] "=&v"(a7),
              [t0] "=&v"(t0), [t1] "=&v"(t1), [t2] "=&v"(t2), [t3] "=&v"(t3),
              [wv] "=&v"(wv), [e] "=&v"(e0), [so] "=&s"(so)
            : [w] "s"(w), [ep] "s"(epoch), [voff] "v"(voff), [rx] "s"(rx), [wp] "v"(wp), [mask] "s"(0xffff0000u)
            : DATA_REGS, "scc", "memory");
    }
    STAMP_AT(1);
    if (t == 0) ((u32*)lds)[L_TAG] = e0 + 1;
    if (t < 576) wl[t] = wv;
    {
        const int r = t / 24, g = t - r * 24;
        *(f32x4*)(lds + L_RED + r * RS + g * 4) = f32x4{a0, a1, a2, a3};
        *(f32x4*)(lds + L_RED + PL + r * RS + g * 4) = f32x4{a4, a5, a6, a7};
    }
    __syncthreads();
    const int q = lane >> 4;
    {
        const int s = 16 * w + (lane & 15);
        const int pl = s >= 96 ? 1 : 0, idx = s - 96 * pl;
        const int c = 8 * (idx >> 2) + 4 * pl + (idx & 3);
        const float* src = lds + L_RED + pl * PL + q * RS + idx;
        float v0 = src[0], v1 = src[4 * RS], v2 = src[8 * RS], v3 = src[12 * RS];
        float v4 = src[16 * RS], v5 = src[20 * RS], v6 = src[24 * RS], v7 = src[28 * RS];
        const unsigned short* wq = wdl + c * R + 3 * q;
        const float w0 = bf2f(wq[0]), w1 = bf2f(wq[1]), w2 = bf2f(wq[2]);
        float sc = ((v0 + v1) + (v2 + v3)) + ((v4 + v5) + (v6 + v7));
        sc = add_xor32(add_xor16(sc));
        float p0 = sc * w0, p1 = sc * w1, p2 = sc * w2;
        p0 = dpp_add<0x111>(p0); p1 = dpp_add<0x111>(p1); p2 = dpp_add<0x111>(p2);
        p0 = dpp_add<0x112>(p0); p1 = dpp_add<0x112>(p1); p2 = dpp_add<0x112>(p2);
        p0 = dpp_add<0x114>(p0); p1 = dpp_add<0x114>(p1); p2 = dpp_add<0x114>(p2);
        p0 = dpp_add<0x118>(p0); p1 = dpp_add<0x118>(p1); p2 = dpp_add<0x118>(p2);
        if ((lane & 15) == 15) {
            float* dst = lds + L_PART + (3 * q) * 16 + w;
            dst[0] = p0; dst[16] = p1; dst[32] = p2;
        }
    }
    __syncthreads();
    const u32 tag = ((const u32*)lds)[L_TAG];
    if (w == 0) {
        f32x4 pv = *(const f32x4*)(lds + L_PART + lane * 4);
        float z = (lane & 3) == 3 ? 0.f : (pv[0] + pv[1]) + (pv[2] + pv[3]);
        z = dpp_add<0xb1>(z);
        z = dpp_add<0x4e>(z);
        if ((lane & 3) == 0)
            __hip_atomic_store(gran + b * 16 + (lane >> 2), ((u64)tag << 32) | __float_as_uint(z), __ATOMIC_RELAXED, __HIP_MEMORY_SCOPE_AGENT);
    }
    STAMP_AT(2);
    float wuv[R];
    if (w < 3) {
#pragma unroll
        for (int j = 0; j < R; ++j) wuv[j] = bf2f(wul[j * C + t]);
    }
    if (w < 8) {
        const auto rg = __builtin_amdgcn_make_buffer_rsrc((void*)(ws + 1024), 0, NWG * 128, 0x00020000);
        const int off = (32 * w + (lane >> 3)) * 128 + (lane & 7) * 16;
        u32x4 g0 = {0, 0, 0, 0}, g1 = g0, g2 = g0, g3 = g0;
        u64 m0 = 0x3f3f3f3f3f3f3f3full, m1 = m0, m2 = m0, m3 = m0;
        if (PRE_SLEEP) __builtin_amdgcn_s_sleep(PRE_SLEEP);
        for (u32 spin = 0; spin < SPIN_CAP; ++spin) {
            asm volatile(
                "s_mov_b64 exec, %[m0]\n\t"
                "s_cbranch_execz 2f\n\t"
                "buffer_load_dwordx4 %[g0], %[off], %[rg], 0 offen sc1\n"
                "2:\n\t"
                "s_mov_b64 exec, %[m1]\n\t"
                "s_cbranch_execz 3f\n\t"
                "buffer_load_dwordx4 %[g1], %[off], %[rg], 0 offen offset:1024 sc1\n"
                "3:\n\t"
                "s_mov_b64 exec, %[m2]\n\t"
                "s_cbranch_execz 4f\n\t"
                "buffer_load_dwordx4 %[g2], %[off], %[rg], 0 offen offset:2048 sc1\n"
                "4:\n\t"
                "s_mov_b64 exec, %[m3]\n\t"
                "s_cbranch_execz 5f\n\t"
                "buffer_load_dwordx4 %[g3], %[off], %[rg], 0 offen offset:3072 sc1\n"
                "5:\n\t"
                "s_mov_b64 exec, -1\n\t"
                "s_waitcnt vmcnt(0)"
                : [g0] "+v"(g0), [g1] "+v"(g1), [g2] "+v"(g2), [g3] "+v"(g3)
                : [m0] "s"(m0), [m1] "s"(m1), [m2] "s"(m2), [m3] "s"(m3), [off] "v"(off), [rg] "s"(rg)
                : "memory");
            m0 &= __ballot((g0[1] != tag) | (g0[3] != tag));
            m1 &= __ballot((g1[1] != tag) | (g1[3] != tag));
            m2 &= __ballot((g2[1] != tag) | (g2[3] != tag));
            m3 &= __ballot((g3[1] != tag) | (g3[3] != tag));
            if ((m0 | m1 | m2 | m3) == 0) break;
            if (POLL_SLEEP) __builtin_amdgcn_s_sleep(POLL_SLEEP);
        }
        float z0 = (__uint_as_float(g0[0]) + __uint_as_float(g1[0])) + (__uint_as_float(g2[0]) + __uint_as_float(g3[0]));
        float z1 = (__uint_as_float(g0[2]) + __uint_as_float(g1[2])) + (__uint_as_float(g2[2]) + __uint_as_float(g3[2]));
        z0 = dpp_add<0x128>(z0); z1 = dpp_add<0x128>(z1);
        z0 = add_xor32(add_xor16(z0)); z1 = add_xor32(add_xor16(z1));
        if (lane < 6) { lds[L_PART2 + (2 * lane) * 8 + w] = z0; lds[L_PART2 + (2 * lane + 1) * 8 + w] = z1; }
    }
    __syncthreads();
    STAMP_AT(3);
    if (b == 0 && t == 0) __hip_atomic_store(epoch, tag, __ATOMIC_RELAXED, __HIP_MEMORY_SCOPE_AGENT);
    if (w < 3) {
        f32x4 pv = *(const f32x4*)(lds + L_PART2 + (lane & 31) * 4);
        float z = (pv[0] + pv[1]) + (pv[2] + pv[3]);
        z = dpp_add<0xb1>(z);
        z = bfround(z * (1.0f / (512.f * 512.f)));
        const int hb = __float_as_int(bfround(z * __builtin_amdgcn_rcpf(1.f + __expf(-z))));
        float u = 0.f;
#pragma unroll
        for (int j = 0; j < R; ++j) u += __int_as_float(__builtin_amdgcn_readlane(hb, 2 * j)) * wuv[j];
        u = bfround(u);
        lds[L_SV + t] = bfround(__builtin_amdgcn_rcpf(1.f + __expf(-u)));
    }
    __syncthreads();
    {
        const int g = t % 24;
        f32x4 sa = *(const f32x4*)(lds + L_SV + g * 8), sb = *(const f32x4*)(lds + L_SV + g * 8 + 4);
        float t0, t1, t2, t3;
        u32 so;
        STAMP_AT(4);
        asm volatile(
            "s_memrealtime s[60:61]\n\t"
            "s_waitcnt lgkmcnt(0)\n\t"
            "s_add_u32 s62, s60, %[phase]\n\t"
            "s_nop 1\n\t"
            ".set se_i, 0\n\t"
            ".rept 32\n\t"
            "v_lshlrev_b32 %[t0], 16, v[40+4*se_i]\n\t"
            "v_and_b32 %[t1], %[mask], v[40+4*se_i]\n\t"
            "v_lshlrev_b32 %[t2], 16, v[41+4*se_i]\n\t"
            "v_and_b32 %[t3], %[mask], v[41+4*se_i]\n\t"
            "v_mul_f32 %[t0], %[t0], %[s0]\n\t"
            "v_mul_f32 %[t1], %[t1], %[s1]\n\t"
            "v_mul_f32 %[t2], %[t2], %[s2]\n\t"
            "v_mul_f32 %[t3], %[t3], %[s3]\n\t"
            "v_cvt_pk_bf16_f32 v[40+4*se_i], %[t0], %[t1]\n\t"
            "v_cvt_pk_bf16_f32 v[41+4*se_i], %[t2], %[t3]\n\t"
            "v_lshlrev_b32 %[t0], 16, v[42+4*se_i]\n\t"
            "v_and_b32 %[t1], %[mask], v[42+4*se_i]\n\t"
            "v_lshlrev_b32 %[t2], 16, v[43+4*se_i]\n\t"
            "v_and_b32 %[t3], %[mask], v[43+4*se_i]\n\t"
            "v_mul_f32 %[t0], %[t0], %[s4]\n\t"
            "v_mul_f32 %[t1], %[t1], %[s5]\n\t"
            "v_mul_f32 %[t2], %[t2], %[s6]\n\t"
            "v_mul_f32 %[t3], %[t3], %[s7]\n\t"
            "s_mov_b32 %[so], se_i*12288\n\t"
            "v_cvt_pk_bf16_f32 v[42+4*se_i], %[t0], %[t1]\n\t"
            "v_cvt_pk_bf16_f32 v[43+4*se_i], %[t2], %[t3]\n\t"
            "s_add_u32 s63, s62, (se_i*" PACE_STR ")/32\n"
            "9:\n\t"
            "s_memrealtime s[60:61]\n\t"
            "s_waitcnt lgkmcnt(0)\n\t"
            "s_sub_u32 s61, s60, s63\n\t"
            "s_cmp_lt_i32 s61, 0\n\t"
            "s_cbranch_scc1 9b\n\t"
            "buffer_store_dwordx4 v[40+4*se_i:43+4*se_i], %[voff], %[ro], %[so] offen" ST_POL "\n\t"
            ".set se_i, se_i+1\n\t"
            ".endr\n\t"
            : [t0] "=&v"(t0), [t1] "=&v"(t1), [t2] "=&v"(t2), [t3] "=&v"(t3), [so] "=&s"(so)
            : [s0] "v"(sa[0]), [s1] "v"(sa[1]), [s2] "v"(sa[2]), [s3] "v"(sa[3]),
              [s4] "v"(sb[0]), [s5] "v"(sb[1]), [s6] "v"(sb[2]), [s7] "v"(sb[3]),
              [voff] "v"(voff), [ro] "s"(ro), [mask] "s"(0xffff0000u), [phase] "s"(w * (PACE / 32) / 12)
            : DATA_REGS, "s60", "s61", "s62", "s63", "scc", "memory");
    }
}

extern "C" void kernel_launch(void* const* d_in, const int* in_sizes, int n_in,
                              void* d_out, int out_size, void* d_ws, size_t ws_size,
                              hipStream_t stream) {
    se_kernel<<<NWG, NT, 0, stream>>>((const u32x4*)d_in[0], (const u32x4*)d_in[1], (const u32x4*)d_in[2],
                                      (u32x4*)d_out, (u32*)d_ws);
}
```

```cpp
#define PROBE_A 4
#define PROBE_B 6
#define PROBE_WG 0
#define PROBE_M 8
#define PROBE_W 11
#include <hip/hip_runtime.h>
#include <hip/hip_bf16.h>
#include <stdint.h>

typedef unsigned int u32;
typedef unsigned long long u64;
typedef u32 u32x4 __attribute__((ext_vector_type(4)));
typedef u32 u32x2 __attribute__((ext_vector_type(2)));
typedef float f32x2 __attribute__((ext_vector_type(2)));
typedef float f32x4 __attribute__((ext_vector_type(4)));
typedef __attribute__((address_space(1))) u32 gu32;
typedef __attribute__((address_space(1))) u64 gu64;

#define C 192
#define R 12
#define NWG 256
#define NT 768
#define NL 32
#define LD_POL " nt"
#define ST_POL " sc0 sc1"
#define SPIN_CAP (1u << 20)
#define REMAP 2
#define PACE 1200
#define STR2(x) #x
#define STR(x) STR2(x)
#define PACE_STR STR(PACE)
#define LEADER 0
#define NLEAD 8
#define PRE_SLEEP 4
#define POLL_SLEEP 0
#define HSLEEP 1

#define CL10(p) "v" #p "0","v" #p "1","v" #p "2","v" #p "3","v" #p "4","v" #p "5","v" #p "6","v" #p "7","v" #p "8","v" #p "9"
#define DATA_REGS CL10(4), CL10(5), CL10(6), CL10(7), CL10(8), CL10(9), CL10(10), CL10(11), CL10(12), CL10(13), CL10(14), CL10(15), \
    "v160","v161","v162","v163","v164","v165","v166","v167"

__device__ __forceinline__ float bf_lo(u32 v) { return __uint_as_float(v << 16); }
__device__ __forceinline__ u32 pack_bf16(float a, float b) {
    typedef __bf16 bf16x2 __attribute__((ext_vector_type(2)));
    f32x2 v = {a, b};
    bf16x2 r = __builtin_convertvector(v, bf16x2);
    return __builtin_bit_cast(u32, r);
}
__device__ __forceinline__ float bfround(float f) { return bf_lo(pack_bf16(f, f)); }
__device__ __forceinline__ float bf2f(unsigned short h) { return __uint_as_float((u32)h << 16); }

template <int CTRL>
__device__ __forceinline__ float dpp_add(float v) {
    return v + __int_as_float(__builtin_amdgcn_update_dpp(0, __float_as_int(v), CTRL, 0xf, 0xf, true));
}
__device__ __forceinline__ float add_xor16(float v) {
    auto r = __builtin_amdgcn_permlane16_swap(__float_as_uint(v), __float_as_uint(v), false, false);
    return __uint_as_float(r[0]) + __uint_as_float(r[1]);
}
__device__ __forceinline__ float add_xor32(float v) {
    auto r = __builtin_amdgcn_permlane32_swap(__float_as_uint(v), __float_as_uint(v), false, false);
    return __uint_as_float(r[0]) + __uint_as_float(r[1]);
}

#define STAMP_AT(k) do { ts[k] = __builtin_amdgcn_s_memrealtime(); } while (0)

#define RS 112
#define PL (32 * RS)
#define L_RED 0
#define L_PART (2 * PL)
#define L_PART2 (L_PART + 256)
#define L_SV (L_PART2 + 128)
#define L_TAG (L_SV + C)
#define L_W (L_TAG + 4)
#define L_END (L_W + 2 * 1152)

__global__ void __launch_bounds__(NT, 3) __attribute__((amdgpu_num_vgpr(20)))
se_kernel(const u32x4* __restrict__ x, const u32x4* __restrict__ wd, const u32x4* __restrict__ wu,
          u32x4* __restrict__ out, u32* __restrict__ ws) {
    __shared__ __attribute__((aligned(16))) float lds[L_END];
    u32x4* wl = (u32x4*)(lds + L_W);
    const unsigned short* wdl = (const unsigned short*)wl;
    const unsigned short* wul = wdl + C * R;

    const int t = threadIdx.x;
    const int lane = t & 63;
    const int w = __builtin_amdgcn_readfirstlane(t >> 6);
    const int b = blockIdx.x;
    u64 ts[8]; u64 ts7 = 0, ts8 = 0; u32 nspin = 0;
    STAMP_AT(0);

    gu32* epoch = (gu32*)ws;
    gu64* hgran = (gu64*)(ws + 64);
    gu64* gran = (gu64*)(ws + 1024);

    const int reg = (b & 7) * 32 + (b >> 3);
    const auto rx = __builtin_amdgcn_make_buffer_rsrc((void*)(x + (size_t)reg * (NT * NL)), 0, NT * NL * 16, 0x00020000);
    const auto ro = __builtin_amdgcn_make_buffer_rsrc((void*)(out + (size_t)reg * (NT * NL)), 0, NT * NL * 16, 0x00020000);
    const int voff = t * 16;
    const int wi = t < 576 ? t : 575;
    const u32x4* wp = wi < 288 ? wd + wi : wu + (wi - 288);

    float a0, a1, a2, a3, a4, a5, a6, a7;
    u32x4 wv;
    u32 e0;
    {
        float t0, t1, t2, t3;
        u32 so;
        asm volatile(
            "s_cmp_lg_u32 %[w], 0\n\t"
            "s_cbranch_scc1 1f\n\t"
            "v_mov_b32 %[e], 0\n\t"
            "global_load_dword %[e], %[e], %[ep] sc1\n"
            "1:\n\t"
            ".set se_i, 0\n\t"
            ".rept 32\n\t"
            "s_mov_b32 %[so], se_i*12288\n\t"
            "buffer_load_dwordx4 v[40+4*se_i:43+4*se_i], %[voff], %[rx], %[so] offen" LD_POL "\n\t"
            ".set se_i, se_i+1\n\t"
            ".endr\n\t"
            "global_load_dwordx4 %[wv], %[wp], off\n\t"
            "v_mov_b32 %[a0], 0\n\tv_mov_b32 %[a1], 0\n\tv_mov_b32 %[a2], 0\n\tv_mov_b32 %[a3], 0\n\t"
            "v_mov_b32 %[a4], 0\n\tv_mov_b32 %[a5], 0\n\tv_mov_b32 %[a6], 0\n\tv_mov_b32 %[a7], 0\n\t"
            ".set se_i, 0\n\t"
            ".rept 32\n\t"
            "s_waitcnt vmcnt(32-se_i)\n\t"
            "v_lshlrev_b32 %[t0], 16, v[40+4*se_i]\n\t"
            "v_and_b32 %[t1], %[mask], v[40+4*se_i]\n\t"
            "v_lshlrev_b32 %[t2], 16, v[41+4*se_i]\n\t"
            "v_and_b32 %[t3], %[mask], v[41+4*se_i]\n\t"
            "v_add_f32 %[a0], %[a0], %[t0]\n\t"
            "v_add_f32 %[a1], %[a1], %[t1]\n\t"
            "v_add_f32 %[a2], %[a2], %[t2]\n\t"
            "v_add_f32 %[a3], %[a3], %[t3]\n\t"
            "v_lshlrev_b32 %[t0], 16, v[42+4*se_i]\n\t"
            "v_and_b32 %[t1], %[mask], v[42+4*se_i]\n\t"
            "v_lshlrev_b32 %[t2], 16, v[43+4*se_i]\n\t"
            "v_and_b32 %[t3], %[mask], v[43+4*se_i]\n\t"
            "v_add_f32 %[a4], %[a4], %[t0]\n\t"
            "v_add_f32 %[a5], %[a5], %[t1]\n\t"
            "v_add_f32 %[a6], %[a6], %[t2]\n\t"
            "v_add_f32 %[a7], %[a7], %[t3]\n\t"
            ".set se_i, se_i+1\n\t"
            ".endr\n\t"
            "s_waitcnt vmcnt(0)\n\t"
            : [a0] "=&v"(a0), [a1] "=&v"(a1), [a2] "=&v"(a2), [a3] "=&v"(a3),
              [a4] "=&v"(a4), [a5] "=&v"(a5), [a6] "=&v"(a6), [a7] "=&v"(a7),
              [t0] "=&v"(t0), [t1] "=&v"(t1), [t2] "=&v"(t2), [t3] "=&v"(t3),
              [wv] "=&v"(wv), [e] "=&v"(e0), [so] "=&s"(so)
            : [w] "s"(w), [ep] "s"(epoch), [voff] "v"(voff), [rx] "s"(rx), [wp] "v"(wp), [mask] "s"(0xffff0000u)
            : DATA_REGS, "scc", "memory");
    }
    STAMP_AT(1);
    if (t == 0) ((u32*)lds)[L_TAG] = e0 + 1;
    if (t < 576) wl[t] = wv;
    {
        const int r = t / 24, g = t - r * 24;
        *(f32x4*)(lds + L_RED + r * RS + g * 4) = f32x4{a0, a1, a2, a3};
        *(f32x4*)(lds + L_RED + PL + r * RS + g * 4) = f32x4{a4, a5, a6, a7};
    }
    __syncthreads();
    const int q = lane >> 4;
    {
        const int s = 16 * w + (lane & 15);
        const int pl = s >= 96 ? 1 : 0, idx = s - 96 * pl;
        const int c = 8 * (idx >> 2) + 4 * pl + (idx & 3);
        const float* src = lds + L_RED + pl * PL + q * RS + idx;
        float v0 = src[0], v1 = src[4 * RS], v2 = src[8 * RS], v3 = src[12 * RS];
        float v4 = src[16 * RS], v5 = src[20 * RS], v6 = src[24 * RS], v7 = src[28 * RS];
        const unsigned short* wq = wdl + c * R + 3 * q;
        const float w0 = bf2f(wq[0]), w1 = bf2f(wq[1]), w2 = bf2f(wq[2]);
        float sc = ((v0 + v1) + (v2 + v3)) + ((v4 + v5) + (v6 + v7));
        sc = add_xor32(add_xor16(sc));
        float p0 = sc * w0, p1 = sc * w1, p2 = sc * w2;
        p0 = dpp_add<0x111>(p0); p1 = dpp_add<0x111>(p1); p2 = dpp_add<0x111>(p2);
        p0 = dpp_add<0x112>(p0); p1 = dpp_add<0x112>(p1); p2 = dpp_add<0x112>(p2);
        p0 = dpp_add<0x114>(p0); p1 = dpp_add<0x114>(p1); p2 = dpp_add<0x114>(p2);
        p0 = dpp_add<0x118>(p0); p1 = dpp_add<0x118>(p1); p2 = dpp_add<0x118>(p2);
        if ((lane & 15) == 15) {
            float* dst = lds + L_PART + (3 * q) * 16 + w;
            dst[0] = p0; dst[16] = p1; dst[32] = p2;
        }
    }
    __syncthreads();
    const u32 tag = ((const u32*)lds)[L_TAG];
    if (w == 0) {
        f32x4 pv = *(const f32x4*)(lds + L_PART + lane * 4);
        float z = (lane & 3) == 3 ? 0.f : (pv[0] + pv[1]) + (pv[2] + pv[3]);
        z = dpp_add<0xb1>(z);
        z = dpp_add<0x4e>(z);
        if ((lane & 3) == 0)
            __hip_atomic_store(gran + b * 16 + (lane >> 2), ((u64)tag << 32) | __float_as_uint(z), __ATOMIC_RELAXED, __HIP_MEMORY_SCOPE_AGENT);
    }
    STAMP_AT(2);
    float wuv[R];
    if (w < 3) {
#pragma unroll
        for (int j = 0; j < R; ++j) wuv[j] = bf2f(wul[j * C + t]);
    }
    if (w < 8) {
        const auto rg = __builtin_amdgcn_make_buffer_rsrc((void*)(ws + 1024), 0, NWG * 128, 0x00020000);
        const int off = (32 * w + (lane >> 3)) * 128 + (lane & 7) * 16;
        u32x4 g0 = {0, 0, 0, 0}, g1 = g0, g2 = g0, g3 = g0;
        u64 m0 = 0x3f3f3f3f3f3f3f3full, m1 = m0, m2 = m0, m3 = m0;
        if (PRE_SLEEP) __builtin_amdgcn_s_sleep(PRE_SLEEP);
        for (u32 spin = 0; spin < SPIN_CAP; ++spin) {
            asm volatile(
                "s_mov_b64 exec, %[m0]\n\t"
                "s_cbranch_execz 2f\n\t"
                "buffer_load_dwordx4 %[g0], %[off], %[rg], 0 offen sc1\n"
                "2:\n\t"
                "s_mov_b64 exec, %[m1]\n\t"
                "s_cbranch_execz 3f\n\t"
                "buffer_load_dwordx4 %[g1], %[off], %[rg], 0 offen offset:1024 sc1\n"
                "3:\n\t"
                "s_mov_b64 exec, %[m2]\n\t"
                "s_cbranch_execz 4f\n\t"
                "buffer_load_dwordx4 %[g2], %[off], %[rg], 0 offen offset:2048 sc1\n"
                "4:\n\t"
                "s_mov_b64 exec, %[m3]\n\t"
                "s_cbranch_execz 5f\n\t"
                "buffer_load_dwordx4 %[g3], %[off], %[rg], 0 offen offset:3072 sc1\n"
                "5:\n\t"
                "s_mov_b64 exec, -1\n\t"
                "s_waitcnt vmcnt(0)"
                : [g0] "+v"(g0), [g1] "+v"(g1), [g2] "+v"(g2), [g3] "+v"(g3)
                : [m0] "s"(m0), [m1] "s"(m1), [m2] "s"(m2), [m3] "s"(m3), [off] "v"(off), [rg] "s"(rg)
                : "memory");
            m0 &= __ballot((g0[1] != tag) | (g0[3] != tag));
            m1 &= __ballot((g1[1] != tag) | (g1[3] != tag));
            m2 &= __ballot((g2[1] != tag) | (g2[3] != tag));
            m3 &= __ballot((g3[1] != tag) | (g3[3] != tag));
            if ((m0 | m1 | m2 | m3) == 0) break;
            if (POLL_SLEEP) __builtin_amdgcn_s_sleep(POLL_SLEEP);
        }
        float z0 = (__uint_as_float(g0[0]) + __uint_as_float(g1[0])) + (__uint_as_float(g2[0]) + __uint_as_float(g3[0]));
        float z1 = (__uint_as_float(g0[2]) + __uint_as_float(g1[2])) + (__uint_as_float(g2[2]) + __uint_as_float(g3[2]));
        z0 = dpp_add<0x128>(z0); z1 = dpp_add<0x128>(z1);
        z0 = add_xor32(add_xor16(z0)); z1 = add_xor32(add_xor16(z1));
        if (lane < 6) { lds[L_PART2 + (2 * lane) * 8 + w] = z0; lds[L_PART2 + (2 * lane + 1) * 8 + w] = z1; }
    }
    __syncthreads();
    STAMP_AT(3);
    if (b == 0 && t == 0) __hip_atomic_store(epoch, tag, __ATOMIC_RELAXED, __HIP_MEMORY_SCOPE_AGENT);
    if (w < 3) {
        f32x4 pv = *(const f32x4*)(lds + L_PART2 + (lane & 31) * 4);
        float z = (pv[0] + pv[1]) + (pv[2] + pv[3]);
        z = dpp_add<0xb1>(z);
        z = bfround(z * (1.0f / (512.f * 512.f)));
        const int hb = __float_as_int(bfround(z * __builtin_amdgcn_rcpf(1.f + __expf(-z))));
        float u = 0.f;
#pragma unroll
        for (int j = 0; j < R; ++j) u += __int_as_float(__builtin_amdgcn_readlane(hb, 2 * j)) * wuv[j];
        u = bfround(u);
        lds[L_SV + t] = bfround(__builtin_amdgcn_rcpf(1.f + __expf(-u)));
    }
    __syncthreads();
    {
        const int g = t % 24;
        f32x4 sa = *(const f32x4*)(lds + L_SV + g * 8), sb = *(const f32x4*)(lds + L_SV + g * 8 + 4);
        float t0, t1, t2, t3;
        u32 so;
        STAMP_AT(4);
        asm volatile(
            "s_memrealtime s[60:61]\n\t"
            "s_waitcnt lgkmcnt(0)\n\t"
            "s_add_u32 s62, s60, %[phase]\n\t"
            "s_nop 1\n\t"
            ".set se_i, 0\n\t"
            ".rept 32\n\t"
            "v_lshlrev_b32 %[t0], 16, v[40+4*se_i]\n\t"
            "v_and_b32 %[t1], %[mask], v[40+4*se_i]\n\t"
            "v_lshlrev_b32 %[t2], 16, v[41+4*se_i]\n\t"
            "v_and_b32 %[t3], %[mask], v[41+4*se_i]\n\t"
            "v_mul_f32 %[t0], %[t0], %[s0]\n\t"
            "v_mul_f32 %[t1], %[t1], %[s1]\n\t"
            "v_mul_f32 %[t2], %[t2], %[s2]\n\t"
            "v_mul_f32 %[t3], %[t3], %[s3]\n\t"
            "v_cvt_pk_bf16_f32 v[40+4*se_i], %[t0], %[t1]\n\t"
            "v_cvt_pk_bf16_f32 v[41+4*se_i], %[t2], %[t3]\n\t"
            "v_lshlrev_b32 %[t0], 16, v[42+4*se_i]\n\t"
            "v_and_b32 %[t1], %[mask], v[42+4*se_i]\n\t"
            "v_lshlrev_b32 %[t2], 16, v[43+4*se_i]\n\t"
            "v_and_b32 %[t3], %[mask], v[43+4*se_i]\n\t"
            "v_mul_f32 %[t0], %[t0], %[s4]\n\t"
            "v_mul_f32 %[t1], %[t1], %[s5]\n\t"
            "v_mul_f32 %[t2], %[t2], %[s6]\n\t"
            "v_mul_f32 %[t3], %[t3], %[s7]\n\t"
            "s_mov_b32 %[so], se_i*12288\n\t"
            "v_cvt_pk_bf16_f32 v[42+4*se_i], %[t0], %[t1]\n\t"
            "v_cvt_pk_bf16_f32 v[43+4*se_i], %[t2], %[t3]\n\t"
            "s_add_u32 s63, s62, (se_i*" PACE_STR ")/32\n"
            "9:\n\t"
            "s_memrealtime s[60:61]\n\t"
            "s_waitcnt lgkmcnt(0)\n\t"
            "s_sub_u32 s61, s60, s63\n\t"
            "s_cmp_lt_i32 s61, 0\n\t"
            "s_cbranch_scc1 9b\n\t"
            "buffer_store_dwordx4 v[40+4*se_i:43+4*se_i], %[voff], %[ro], %[so] offen" ST_POL "\n\t"
            ".set se_i, se_i+1\n\t"
            ".endr\n\t"
            : [t0] "=&v"(t0), [t1] "=&v"(t1), [t2] "=&v"(t2), [t3] "=&v"(t3), [so] "=&s"(so)
            : [s0] "v"(sa[0]), [s1] "v"(sa[1]), [s2] "v"(sa[2]), [s3] "v"(sa[3]),
              [s4] "v"(sb[0]), [s5] "v"(sb[1]), [s6] "v"(sb[2]), [s7] "v"(sb[3]),
              [voff] "v"(voff), [ro] "s"(ro), [mask] "s"(0xffff0000u), [phase] "s"(w * (PACE / 32) / 12)
            : DATA_REGS, "s60", "s61", "s62", "s63", "scc", "memory");
    }
    STAMP_AT(5);
    asm volatile("s_waitcnt vmcnt(0)" ::: "memory");
    STAMP_AT(6);
    if (b == PROBE_WG && w == PROBE_W) {
        const u64 d = (ts[PROBE_B] - ts[PROBE_A]) * PROBE_M;
        const u64 t1 = __builtin_amdgcn_s_memrealtime();
        while (__builtin_amdgcn_s_memrealtime() - t1 < d) __builtin_amdgcn_s_sleep(2);
    }
}

extern "C" void kernel_launch(void* const* d_in, const int* in_sizes, int n_in,
                              void* d_out, int out_size, void* d_ws, size_t ws_size,
                              hipStream_t stream) {
    se_kernel<<<NWG, NT, 0, stream>>>((const u32x4*)d_in[0], (const u32x4*)d_in[1], (const u32x4*)d_in[2],
                                      (u32x4*)d_out, (u32*)d_ws);
}
```

```cpp
#include <hip/hip_runtime.h>
#include <hip/hip_bf16.h>
#include <stdint.h>

typedef unsigned int u32;
typedef unsigned long long u64;
typedef u32 u32x4 __attribute__((ext_vector_type(4)));
typedef u32 u32x2 __attribute__((ext_vector_type(2)));
typedef float f32x2 __attribute__((ext_vector_type(2)));
typedef float f32x4 __attribute__((ext_vector_type(4)));
typedef __attribute__((address_space(1))) u32 gu32;
typedef __attribute__((address_space(1))) u64 gu64;

#define C 192
#define R 12
#define NWG 256
#define NT 768
#define NL 32
#define LD_POL " nt"
#define ST_POL " sc0 sc1"
#define SPIN_CAP (1u << 20)
#define REMAP 2
#define LPACE 1
#define LAG 8
#define LBURST 1
#define LAYOUT 6
#define TIMECHK 0
#define PACE 0
#define STR2(x) #x
#define STR(x) STR2(x)
#define PACE_STR STR(PACE)
#define LEADER 0
#define NLEAD 8
#define PRE_SLEEP 4
#define POLL_SLEEP 0
#define HSLEEP 1

#define CL10(p) "v" #p "0","v" #p "1","v" #p "2","v" #p "3","v" #p "4","v" #p "5","v" #p "6","v" #p "7","v" #p "8","v" #p "9"
#define DATA_REGS CL10(4), CL10(5), CL10(6), CL10(7), CL10(8), CL10(9), CL10(10), CL10(11), CL10(12), CL10(13), CL10(14), CL10(15), \
    "v160","v161","v162","v163","v164","v165","v166","v167"

__device__ __forceinline__ float bf_lo(u32 v) { return __uint_as_float(v << 16); }
__device__ __forceinline__ u32 pack_bf16(float a, float b) {
    typedef __bf16 bf16x2 __attribute__((ext_vector_type(2)));
    f32x2 v = {a, b};
    bf16x2 r = __builtin_convertvector(v, bf16x2);
    return __builtin_bit_cast(u32, r);
}
__device__ __forceinline__ float bfround(float f) { return bf_lo(pack_bf16(f, f)); }
__device__ __forceinline__ float bf2f(unsigned short h) { return __uint_as_float((u32)h << 16); }

template <int CTRL>
__device__ __forceinline__ float dpp_add(float v) {
    return v + __int_as_float(__builtin_amdgcn_update_dpp(0, __float_as_int(v), CTRL, 0xf, 0xf, true));
}
__device__ __forceinline__ float add_xor16(float v) {
    auto r = __builtin_amdgcn_permlane16_swap(__float_as_uint(v), __float_as_uint(v), false, false);
    return __uint_as_float(r[0]) + __uint_as_float(r[1]);
}
__device__ __forceinline__ float add_xor32(float v) {
    auto r = __builtin_amdgcn_permlane32_swap(__float_as_uint(v), __float_as_uint(v), false, false);
    return __uint_as_float(r[0]) + __uint_as_float(r[1]);
}

#define STAMP_AT(k) do { } while (0)

#define RS 112
#define PL (32 * RS)
#define L_RED 0
#define L_PART (2 * PL)
#define L_PART2 (L_PART + 256)
#define L_SV (L_PART2 + 128)
#define L_TAG (L_SV + C)
#define L_W (L_TAG + 4)
#define L_END (L_W + 2 * 1152)

__global__ void __launch_bounds__(NT, 3) __attribute__((amdgpu_num_vgpr(20)))
se_kernel(const u32x4* __restrict__ x, const u32x4* __restrict__ wd, const u32x4* __restrict__ wu,
          u32x4* __restrict__ out, u32* __restrict__ ws) {
    __shared__ __attribute__((aligned(16))) float lds[L_END];
    u32x4* wl = (u32x4*)(lds + L_W);
    const unsigned short* wdl = (const unsigned short*)wl;
    const unsigned short* wul = wdl + C * R;

    const int t = threadIdx.x;
    const int lane = t & 63;
    const int w = __builtin_amdgcn_readfirstlane(t >> 6);
    const int b = blockIdx.x;
    STAMP_AT(0);

    gu32* epoch = (gu32*)ws;
    gu64* hgran = (gu64*)(ws + 64);
    gu64* gran = (gu64*)(ws + 1024);

    const int reg = (b & 7) * 32 + (b >> 3);
    const size_t base = (size_t)reg * NT;
#define STEP 3145728
    const u32 span = 31u * STEP + NT * 16;
    const auto rx = __builtin_amdgcn_make_buffer_rsrc((void*)(x + base), 0, span, 0x00020000);
    const auto ro = __builtin_amdgcn_make_buffer_rsrc((void*)(out + base), 0, span, 0x00020000);
    const int voff = t * 16;
    const int wi = t < 576 ? t : 575;
    const u32x4* wp = wi < 288 ? wd + wi : wu + (wi - 288);

    float a0, a1, a2, a3, a4, a5, a6, a7;
    u32x4 wv;
    u32 e0;
    {
        float t0, t1, t2, t3;
        u32 so;
        asm volatile(
            "s_cmp_lg_u32 %[w], 0\n\t"
            "s_cbranch_scc1 1f\n\t"
            "v_mov_b32 %[e], 0\n\t"
            "global_load_dword %[e], %[e], %[ep] sc1\n"
            "1:\n\t"
            "s_memrealtime s[60:61]\n\t"
            "global_load_dwordx4 %[wv], %[wp], off\n\t"
            "v_mov_b32 %[a0], 0\n\tv_mov_b32 %[a1], 0\n\tv_mov_b32 %[a2], 0\n\tv_mov_b32 %[a3], 0\n\t"
            "v_mov_b32 %[a4], 0\n\tv_mov_b32 %[a5], 0\n\tv_mov_b32 %[a6], 0\n\tv_mov_b32 %[a7], 0\n\t"
            "s_waitcnt lgkmcnt(0)\n\t"
            "s_add_u32 s62, s60, %[lphase]\n\t"
            ".set se_i, 0\n\t"
            ".rept 32\n\t"
            "s_mov_b32 %[so], se_i*" STR(STEP) "\n\t"
            "buffer_load_dwordx4 v[40+4*se_i:43+4*se_i], %[voff], %[rx], %[so] offen" LD_POL "\n\t"
            ".if se_i >= " STR(LAG) "\n\t"
            ".set se_k, se_i-" STR(LAG) "\n\t"
            "s_waitcnt vmcnt(" STR(LAG) ")\n\t"
            "v_lshlrev_b32 %[t0], 16, v[40+4*se_k]\n\t"
            "v_and_b32 %[t1], %[mask], v[40+4*se_k]\n\t"
            "v_lshlrev_b32 %[t2], 16, v[41+4*se_k]\n\t"
            "v_and_b32 %[t3], %[mask], v[41+4*se_k]\n\t"
            "v_add_f32 %[a0], %[a0], %[t0]\n\t"
            "v_add_f32 %[a1], %[a1], %[t1]\n\t"
            "v_add_f32 %[a2], %[a2], %[t2]\n\t"
            "v_add_f32 %[a3], %[a3], %[t3]\n\t"
            "v_lshlrev_b32 %[t0], 16, v[42+4*se_k]\n\t"
            "v_and_b32 %[t1], %[mask], v[42+4*se_k]\n\t"
            "v_lshlrev_b32 %[t2], 16, v[43+4*se_k]\n\t"
            "v_and_b32 %[t3], %[mask], v[43+4*se_k]\n\t"
            "v_add_f32 %[a4], %[a4], %[t0]\n\t"
            "v_add_f32 %[a5], %[a5], %[t1]\n\t"
            "v_add_f32 %[a6], %[a6], %[t2]\n\t"
            "v_add_f32 %[a7], %[a7], %[t3]\n\t"
            ".endif\n\t"
            ".set se_i, se_i+1\n\t"
            ".endr\n\t"
            ".set se_k, 32-" STR(LAG) "\n\t"
            ".rept " STR(LAG) "\n\t"
            "s_waitcnt vmcnt(31-se_k)\n\t"
            "v_lshlrev_b32 %[t0], 16, v[40+4*se_k]\n\t"
            "v_and_b32 %[t1], %[mask], v[40+4*se_k]\n\t"
            "v_lshlrev_b32 %[t2], 16, v[41+4*se_k]\n\t"
            "v_and_b32 %[t3], %[mask], v[41+4*se_k]\n\t"
            "v_add_f32 %[a0], %[a0], %[t0]\n\t"
            "v_add_f32 %[a1], %[a1], %[t1]\n\t"
            "v_add_f32 %[a2], %[a2], %[t2]\n\t"
            "v_add_f32 %[a3], %[a3], %[t3]\n\t"
            "v_lshlrev_b32 %[t0], 16, v[42+4*se_k]\n\t"
            "v_and_b32 %[t1], %[mask], v[42+4*se_k]\n\t"
            "v_lshlrev_b32 %[t2], 16, v[43+4*se_k]\n\t"
            "v_and_b32 %[t3], %[mask], v[43+4*se_k]\n\t"
            "v_add_f32 %[a4], %[a4], %[t0]\n\t"
            "v_add_f32 %[a5], %[a5], %[t1]\n\t"
            "v_add_f32 %[a6], %[a6], %[t2]\n\t"
            "v_add_f32 %[a7], %[a7], %[t3]\n\t"
            ".set se_k, se_k+1\n\t"
            ".endr\n\t"
            "s_waitcnt vmcnt(0)\n\t"
            : [a0] "=&v"(a0), [a1] "=&v"(a1), [a2] "=&v"(a2), [a3] "=&v"(a3),
              [a4] "=&v"(a4), [a5] "=&v"(a5), [a6] "=&v"(a6), [a7] "=&v"(a7),
              [t0] "=&v"(t0), [t1] "=&v"(t1), [t2] "=&v"(t2), [t3] "=&v"(t3),
              [wv] "=&v"(wv), [e] "=&v"(e0), [so] "=&s"(so)
            : [w] "s"(w), [ep] "s"(epoch), [voff] "v"(voff), [rx] "s"(rx), [wp] "v"(wp), [mask] "s"(0xffff0000u), [lphase] "s"(w * (LPACE / 32) / 12)
            : DATA_REGS, "s60", "s61", "s62", "s63", "scc", "memory");
    }
    STAMP_AT(1);
    if (t == 0) ((u32*)lds)[L_TAG] = e0 + 1;
    if (t < 576) wl[t] = wv;
    {
        const int r = t / 24, g = t - r * 24;
        *(f32x4*)(lds + L_RED + r * RS + g * 4) = f32x4{a0, a1, a2, a3};
        *(f32x4*)(lds + L_RED + PL + r * RS + g * 4) = f32x4{a4, a5, a6, a7};
    }
    __syncthreads();
    const int q = lane >> 4;
    {
        const int s = 16 * w + (lane & 15);
        const int pl = s >= 96 ? 1 : 0, idx = s - 96 * pl;
        const int c = 8 * (idx >> 2) + 4 * pl + (idx & 3);
        const float* src = lds + L_RED + pl * PL + q * RS + idx;
        float v0 = src[0], v1 = src[4 * RS], v2 = src[8 * RS], v3 = src[12 * RS];
        float v4 = src[16 * RS], v5 = src[20 * RS], v6 = src[24 * RS], v7 = src[28 * RS];
        const unsigned short* wq = wdl + c * R + 3 * q;
        const float w0 = bf2f(wq[0]), w1 = bf2f(wq[1]), w2 = bf2f(wq[2]);
        float sc = ((v0 + v1) + (v2 + v3)) + ((v4 + v5) + (v6 + v7));
        sc = add_xor32(add_xor16(sc));
        float p0 = sc * w0, p1 = sc * w1, p2 = sc * w2;
        p0 = dpp_add<0x111>(p0); p1 = dpp_add<0x111>(p1); p2 = dpp_add<0x111>(p2);
        p0 = dpp_add<0x112>(p0); p1 = dpp_add<0x112>(p1); p2 = dpp_add<0x112>(p2);
        p0 = dpp_add<0x114>(p0); p1 = dpp_add<0x114>(p1); p2 = dpp_add<0x114>(p2);
        p0 = dpp_add<0x118>(p0); p1 = dpp_add<0x118>(p1); p2 = dpp_add<0x118>(p2);
        if ((lane & 15) == 15) {
            float* dst = lds + L_PART + (3 * q) * 16 + w;
            dst[0] = p0; dst[16] = p1; dst[32] = p2;
        }
    }
    __syncthreads();
    const u32 tag = ((const u32*)lds)[L_TAG];
    if (w == 0) {
        f32x4 pv = *(const f32x4*)(lds + L_PART + lane * 4);
        float z = (lane & 3) == 3 ? 0.f : (pv[0] + pv[1]) + (pv[2] + pv[3]);
        z = dpp_add<0xb1>(z);
        z = dpp_add<0x4e>(z);
        if ((lane & 3) == 0)
            __hip_atomic_store(gran + b * 16 + (lane >> 2), ((u64)tag << 32) | __float_as_uint(z), __ATOMIC_RELAXED, __HIP_MEMORY_SCOPE_AGENT);
    }
    STAMP_AT(2);
    float wuv[R];
    if (w < 3) {
#pragma unroll
        for (int j = 0; j < R; ++j) wuv[j] = bf2f(wul[j * C + t]);
    }
    if (w < 8) {
        const auto rg = __builtin_amdgcn_make_buffer_rsrc((void*)(ws + 1024), 0, NWG * 128, 0x00020000);
        const int off = (32 * w + (lane >> 3)) * 128 + (lane & 7) * 16;
        u32x4 g0 = {0, 0, 0, 0}, g1 = g0, g2 = g0, g3 = g0;
        u64 m0 = 0x3f3f3f3f3f3f3f3full, m1 = m0, m2 = m0, m3 = m0;
        if (PRE_SLEEP) __builtin_amdgcn_s_sleep(PRE_SLEEP);
        for (u32 spin = 0; spin < SPIN_CAP; ++spin) {
            asm volatile(
                "s_mov_b64 exec, %[m0]\n\t"
                "s_cbranch_execz 2f\n\t"
                "buffer_load_dwordx4 %[g0], %[off], %[rg], 0 offen sc1\n"
                "2:\n\t"
                "s_mov_b64 exec, %[m1]\n\t"
                "s_cbranch_execz 3f\n\t"
                "buffer_load_dwordx4 %[g1], %[off], %[rg], 0 offen offset:1024 sc1\n"
                "3:\n\t"
                "s_mov_b64 exec, %[m2]\n\t"
                "s_cbranch_execz 4f\n\t"
                "buffer_load_dwordx4 %[g2], %[off], %[rg], 0 offen offset:2048 sc1\n"
                "4:\n\t"
                "s_mov_b64 exec, %[m3]\n\t"
                "s_cbranch_execz 5f\n\t"
                "buffer_load_dwordx4 %[g3], %[off], %[rg], 0 offen offset:3072 sc1\n"
                "5:\n\t"
                "s_mov_b64 exec, -1\n\t"
                "s_waitcnt vmcnt(0)"
                : [g0] "+v"(g0), [g1] "+v"(g1), [g2] "+v"(g2), [g3] "+v"(g3)
                : [m0] "s"(m0), [m1] "s"(m1), [m2] "s"(m2), [m3] "s"(m3), [off] "v"(off), [rg] "s"(rg)
                : "memory");
            m0 &= __ballot((g0[1] != tag) | (g0[3] != tag));
            m1 &= __ballot((g1[1] != tag) | (g1[3] != tag));
            m2 &= __ballot((g2[1] != tag) | (g2[3] != tag));
            m3 &= __ballot((g3[1] != tag) | (g3[3] != tag));
            if ((m0 | m1 | m2 | m3) == 0) break;
            if (POLL_SLEEP) __builtin_amdgcn_s_sleep(POLL_SLEEP);
        }
        float z0 = (__uint_as_float(g0[0]) + __uint_as_float(g1[0])) + (__uint_as_float(g2[0]) + __uint_as_float(g3[0]));
        float z1 = (__uint_as_float(g0[2]) + __uint_as_float(g1[2])) + (__uint_as_float(g2[2]) + __uint_as_float(g3[2]));
        z0 = dpp_add<0x128>(z0); z1 = dpp_add<0x128>(z1);
        z0 = add_xor32(add_xor16(z0)); z1 = add_xor32(add_xor16(z1));
        if (lane < 6) { lds[L_PART2 + (2 * lane) * 8 + w] = z0; lds[L_PART2 + (2 * lane + 1) * 8 + w] = z1; }
    }
    __syncthreads();
    STAMP_AT(3);
    if (b == 0 && t == 0) __hip_atomic_store(epoch, tag, __ATOMIC_RELAXED, __HIP_MEMORY_SCOPE_AGENT);
    if (w < 3) {
        f32x4 pv = *(const f32x4*)(lds + L_PART2 + (lane & 31) * 4);
        float z = (pv[0] + pv[1]) + (pv[2] + pv[3]);
        z = dpp_add<0xb1>(z);
        z = bfround(z * (1.0f / (512.f * 512.f)));
        const int hb = __float_as_int(bfround(z * __builtin_amdgcn_rcpf(1.f + __expf(-z))));
        float u = 0.f;
#pragma unroll
        for (int j = 0; j < R; ++j) u += __int_as_float(__builtin_amdgcn_readlane(hb, 2 * j)) * wuv[j];
        u = bfround(u);
        lds[L_SV + t] = bfround(__builtin_amdgcn_rcpf(1.f + __expf(-u)));
    }
    __syncthreads();
    {
        const int g = t % 24;
        f32x4 sa = *(const f32x4*)(lds + L_SV + g * 8), sb = *(const f32x4*)(lds + L_SV + g * 8 + 4);
        float t0, t1, t2, t3;
        u32 so;
        STAMP_AT(4);
        asm volatile(
            "s_nop 1\n\t"
            ".set se_i, 0\n\t"
            ".rept 32\n\t"
            "v_lshlrev_b32 %[t0], 16, v[40+4*se_i]\n\t"
            "v_and_b32 %[t1], %[mask], v[40+4*se_i]\n\t"
            "v_lshlrev_b32 %[t2], 16, v[41+4*se_i]\n\t"
            "v_and_b32 %[t3], %[mask], v[41+4*se_i]\n\t"
            "v_mul_f32 %[t0], %[t0], %[s0]\n\t"
            "v_mul_f32 %[t1], %[t1], %[s1]\n\t"
            "v_mul_f32 %[t2], %[t2], %[s2]\n\t"
            "v_mul_f32 %[t3], %[t3], %[s3]\n\t"
            "v_cvt_pk_bf16_f32 v[40+4*se_i], %[t0], %[t1]\n\t"
            "v_cvt_pk_bf16_f32 v[41+4*se_i], %[t2], %[t3]\n\t"
            "v_lshlrev_b32 %[t0], 16, v[42+4*se_i]\n\t"
            "v_and_b32 %[t1], %[mask], v[42+4*se_i]\n\t"
            "v_lshlrev_b32 %[t2], 16, v[43+4*se_i]\n\t"
            "v_and_b32 %[t3], %[mask], v[43+4*se_i]\n\t"
            "v_mul_f32 %[t0], %[t0], %[s4]\n\t"
            "v_mul_f32 %[t1], %[t1], %[s5]\n\t"
            "v_mul_f32 %[t2], %[t2], %[s6]\n\t"
            "v_mul_f32 %[t3], %[t3], %[s7]\n\t"
            "s_mov_b32 %[so], se_i*" STR(STEP) "\n\t"
            "v_cvt_pk_bf16_f32 v[42+4*se_i], %[t0], %[t1]\n\t"
            "v_cvt_pk_bf16_f32 v[43+4*se_i], %[t2], %[t3]\n\t"
            "buffer_store_dwordx4 v[40+4*se_i:43+4*se_i], %[voff], %[ro], %[so] offen" ST_POL "\n\t"
            ".set se_i, se_i+1\n\t"
            ".endr\n\t"
            : [t0] "=&v"(t0), [t1] "=&v"(t1), [t2] "=&v"(t2), [t3] "=&v"(t3), [so] "=&s"(so)
            : [s0] "v"(sa[0]), [s1] "v"(sa[1]), [s2] "v"(sa[2]), [s3] "v"(sa[3]),
              [s4] "v"(sb[0]), [s5] "v"(sb[1]), [s6] "v"(sb[2]), [s7] "v"(sb[3]),
              [voff] "v"(voff), [ro] "s"(ro), [mask] "s"(0xffff0000u), [phase] "s"(w * (PACE / 32) / 12)
            : DATA_REGS, "s60", "s61", "s62", "s63", "scc", "memory");
    }
}

extern "C" void kernel_launch(void* const* d_in, const int* in_sizes, int n_in,
                              void* d_out, int out_size, void* d_ws, size_t ws_size,
                              hipStream_t stream) {
    se_kernel<<<NWG, NT, 0, stream>>>((const u32x4*)d_in[0], (const u32x4*)d_in[1], (const u32x4*)d_in[2],
                                      (u32x4*)d_out, (u32*)d_ws);
}
```

```cpp
#include <hip/hip_runtime.h>
#include <hip/hip_bf16.h>
#include <stdint.h>

typedef unsigned int u32;
typedef unsigned long long u64;
typedef u32 u32x4 __attribute__((ext_vector_type(4)));
typedef u32 u32x2 __attribute__((ext_vector_type(2)));
typedef float f32x2 __attribute__((ext_vector_type(2)));
typedef float f32x4 __attribute__((ext_vector_type(4)));
typedef __attribute__((address_space(1))) u32 gu32;
typedef __attribute__((address_space(1))) u64 gu64;

#define C 192
#define R 12
#define NWG 256
#define NT 768
#define NL 32
#define LD_POL " nt"
#define ST_POL " sc0 sc1"
#define SPIN_CAP (1u << 20)
#define REMAP 2
#define LPACE 1
#define LAG 8
#define LBURST 1
#define LAYOUT 6
#define TIMECHK 0
#define PWHO 0
#define PACE 1240
#define STR2(x) #x
#define STR(x) STR2(x)
#define PACE_STR STR(PACE)
#define LEADER 0
#define NLEAD 8
#define PRE_SLEEP 4
#define POLL_SLEEP 0
#define HSLEEP 1

#define CL10(p) "v" #p "0","v" #p "1","v" #p "2","v" #p "3","v" #p "4","v" #p "5","v" #p "6","v" #p "7","v" #p "8","v" #p "9"
#define DATA_REGS CL10(4), CL10(5), CL10(6), CL10(7), CL10(8), CL10(9), CL10(10), CL10(11), CL10(12), CL10(13), CL10(14), CL10(15), \
    "v160","v161","v162","v163","v164","v165","v166","v167"

__device__ __forceinline__ float bf_lo(u32 v) { return __uint_as_float(v << 16); }
__device__ __forceinline__ u32 pack_bf16(float a, float b) {
    typedef __bf16 bf16x2 __attribute__((ext_vector_type(2)));
    f32x2 v = {a, b};
    bf16x2 r = __builtin_convertvector(v, bf16x2);
    return __builtin_bit_cast(u32, r);
}
__device__ __forceinline__ float bfround(float f) { return bf_lo(pack_bf16(f, f)); }
__device__ __forceinline__ float bf2f(unsigned short h) { return __uint_as_float((u32)h << 16); }

template <int CTRL>
__device__ __forceinline__ float dpp_add(float v) {
    return v + __int_as_float(__builtin_amdgcn_update_dpp(0, __float_as_int(v), CTRL, 0xf, 0xf, true));
}
__device__ __forceinline__ float add_xor16(float v) {
    auto r = __builtin_amdgcn_permlane16_swap(__float_as_uint(v), __float_as_uint(v), false, false);
    return __uint_as_float(r[0]) + __uint_as_float(r[1]);
}
__device__ __forceinline__ float add_xor32(float v) {
    auto r = __builtin_amdgcn_permlane32_swap(__float_as_uint(v), __float_as_uint(v), false, false);
    return __uint_as_float(r[0]) + __uint_as_float(r[1]);
}

#define STAMP_AT(k) do { } while (0)

#define RS 112
#define PL (32 * RS)
#define L_RED 0
#define L_PART (2 * PL)
#define L_PART2 (L_PART + 256)
#define L_SV (L_PART2 + 128)
#define L_TAG (L_SV + C)
#define L_W (L_TAG + 4)
#define L_END (L_W + 2 * 1152)

__global__ void __launch_bounds__(NT, 3) __attribute__((amdgpu_num_vgpr(20)))
se_kernel(const u32x4* __restrict__ x, const u32x4* __restrict__ wd, const u32x4* __restrict__ wu,
          u32x4* __restrict__ out, u32* __restrict__ ws) {
    __shared__ __attribute__((aligned(16))) float lds[L_END];
    u32x4* wl = (u32x4*)(lds + L_W);
    const unsigned short* wdl = (const unsigned short*)wl;
    const unsigned short* wul = wdl + C * R;

    const int t = threadIdx.x;
    const int lane = t & 63;
    const int w = __builtin_amdgcn_readfirstlane(t >> 6);
    const int b = blockIdx.x;
    STAMP_AT(0);

    gu32* epoch = (gu32*)ws;
    gu64* hgran = (gu64*)(ws + 64);
    gu64* gran = (gu64*)(ws + 1024);

    const int reg = (b & 7) * 32 + (b >> 3);
    const size_t base = (size_t)reg * NT;
#define STEP 3145728
    const u32 span = 31u * STEP + NT * 16;
    const auto rx = __builtin_amdgcn_make_buffer_rsrc((void*)(x + base), 0, span, 0x00020000);
    const auto ro = __builtin_amdgcn_make_buffer_rsrc((void*)(out + base), 0, span, 0x00020000);
    const int voff = t * 16;
    const int wi = t < 576 ? t : 575;
    const u32x4* wp = wi < 288 ? wd + wi : wu + (wi - 288);

    float a0, a1, a2, a3, a4, a5, a6, a7;
    u32x4 wv;
    u32 e0;
    {
        float t0, t1, t2, t3;
        u32 so;
        asm volatile(
            "s_cmp_lg_u32 %[w], 0\n\t"
            "s_cbranch_scc1 1f\n\t"
            "v_mov_b32 %[e], 0\n\t"
            "global_load_dword %[e], %[e], %[ep] sc1\n"
            "1:\n\t"
            "s_memrealtime s[60:61]\n\t"
            "global_load_dwordx4 %[wv], %[wp], off\n\t"
            "v_mov_b32 %[a0], 0\n\tv_mov_b32 %[a1], 0\n\tv_mov_b32 %[a2], 0\n\tv_mov_b32 %[a3], 0\n\t"
            "v_mov_b32 %[a4], 0\n\tv_mov_b32 %[a5], 0\n\tv_mov_b32 %[a6], 0\n\tv_mov_b32 %[a7], 0\n\t"
            "s_waitcnt lgkmcnt(0)\n\t"
            "s_add_u32 s62, s60, %[lphase]\n\t"
            ".set se_i, 0\n\t"
            ".rept 32\n\t"
            "s_mov_b32 %[so], se_i*" STR(STEP) "\n\t"
            "buffer_load_dwordx4 v[40+4*se_i:43+4*se_i], %[voff], %[rx], %[so] offen" LD_POL "\n\t"
            ".if se_i >= " STR(LAG) "\n\t"
            ".set se_k, se_i-" STR(LAG) "\n\t"
            "s_waitcnt vmcnt(" STR(LAG) ")\n\t"
            "v_lshlrev_b32 %[t0], 16, v[40+4*se_k]\n\t"
            "v_and_b32 %[t1], %[mask], v[40+4*se_k]\n\t"
            "v_lshlrev_b32 %[t2], 16, v[41+4*se_k]\n\t"
            "v_and_b32 %[t3], %[mask], v[41+4*se_k]\n\t"
            "v_add_f32 %[a0], %[a0], %[t0]\n\t"
            "v_add_f32 %[a1], %[a1], %[t1]\n\t"
            "v_add_f32 %[a2], %[a2], %[t2]\n\t"
            "v_add_f32 %[a3], %[a3], %[t3]\n\t"
            "v_lshlrev_b32 %[t0], 16, v[42+4*se_k]\n\t"
            "v_and_b32 %[t1], %[mask], v[42+4*se_k]\n\t"
            "v_lshlrev_b32 %[t2], 16, v[43+4*se_k]\n\t"
            "v_and_b32 %[t3], %[mask], v[43+4*se_k]\n\t"
            "v_add_f32 %[a4], %[a4], %[t0]\n\t"
            "v_add_f32 %[a5], %[a5], %[t1]\n\t"
            "v_add_f32 %[a6], %[a6], %[t2]\n\t"
            "v_add_f32 %[a7], %[a7], %[t3]\n\t"
            ".endif\n\t"
            ".set se_i, se_i+1\n\t"
            ".endr\n\t"
            ".set se_k, 32-" STR(LAG) "\n\t"
            ".rept " STR(LAG) "\n\t"
            "s_waitcnt vmcnt(31-se_k)\n\t"
            "v_lshlrev_b32 %[t0], 16, v[40+4*se_k]\n\t"
            "v_and_b32 %[t1], %[mask], v[40+4*se_k]\n\t"
            "v_lshlrev_b32 %[t2], 16, v[41+4*se_k]\n\t"
            "v_and_b32 %[t3], %[mask], v[41+4*se_k]\n\t"
            "v_add_f32 %[a0], %[a0], %[t0]\n\t"
            "v_add_f32 %[a1], %[a1], %[t1]\n\t"
            "v_add_f32 %[a2], %[a2], %[t2]\n\t"
            "v_add_f32 %[a3], %[a3], %[t3]\n\t"
            "v_lshlrev_b32 %[t0], 16, v[42+4*se_k]\n\t"
            "v_and_b32 %[t1], %[mask], v[42+4*se_k]\n\t"
            "v_lshlrev_b32 %[t2], 16, v[43+4*se_k]\n\t"
            "v_and_b32 %[t3], %[mask], v[43+4*se_k]\n\t"
            "v_add_f32 %[a4], %[a4], %[t0]\n\t"
            "v_add_f32 %[a5], %[a5], %[t1]\n\t"
            "v_add_f32 %[a6], %[a6], %[t2]\n\t"
            "v_add_f32 %[a7], %[a7], %[t3]\n\t"
            ".set se_k, se_k+1\n\t"
            ".endr\n\t"
            "s_waitcnt vmcnt(0)\n\t"
            : [a0] "=&v"(a0), [a1] "=&v"(a1), [a2] "=&v"(a2), [a3] "=&v"(a3),
              [a4] "=&v"(a4), [a5] "=&v"(a5), [a6] "=&v"(a6), [a7] "=&v"(a7),
              [t0] "=&v"(t0), [t1] "=&v"(t1), [t2] "=&v"(t2), [t3] "=&v"(t3),
              [wv] "=&v"(wv), [e] "=&v"(e0), [so] "=&s"(so)
            : [w] "s"(w), [ep] "s"(epoch), [voff] "v"(voff), [rx] "s"(rx), [wp] "v"(wp), [mask] "s"(0xffff0000u), [lphase] "s"(w * (LPACE / 32) / 12)
            : DATA_REGS, "s60", "s61", "s62", "s63", "scc", "memory");
    }
    STAMP_AT(1);
    if (t == 0) ((u32*)lds)[L_TAG] = e0 + 1;
    if (t < 576) wl[t] = wv;
    {
        const int r = t / 24, g = t - r * 24;
        *(f32x4*)(lds + L_RED + r * RS + g * 4) = f32x4{a0, a1, a2, a3};
        *(f32x4*)(lds + L_RED + PL + r * RS + g * 4) = f32x4{a4, a5, a6, a7};
    }
    __syncthreads();
    const int q = lane >> 4;
    {
        const int s = 16 * w + (lane & 15);
        const int pl = s >= 96 ? 1 : 0, idx = s - 96 * pl;
        const int c = 8 * (idx >> 2) + 4 * pl + (idx & 3);
        const float* src = lds + L_RED + pl * PL + q * RS + idx;
        float v0 = src[0], v1 = src[4 * RS], v2 = src[8 * RS], v3 = src[12 * RS];
        float v4 = src[16 * RS], v5 = src[20 * RS], v6 = src[24 * RS], v7 = src[28 * RS];
        const unsigned short* wq = wdl + c * R + 3 * q;
        const float w0 = bf2f(wq[0]), w1 = bf2f(wq[1]), w2 = bf2f(wq[2]);
        float sc = ((v0 + v1) + (v2 + v3)) + ((v4 + v5) + (v6 + v7));
        sc = add_xor32(add_xor16(sc));
        float p0 = sc * w0, p1 = sc * w1, p2 = sc * w2;
        p0 = dpp_add<0x111>(p0); p1 = dpp_add<0x111>(p1); p2 = dpp_add<0x111>(p2);
        p0 = dpp_add<0x112>(p0); p1 = dpp_add<0x112>(p1); p2 = dpp_add<0x112>(p2);
        p0 = dpp_add<0x114>(p0); p1 = dpp_add<0x114>(p1); p2 = dpp_add<0x114>(p2);
        p0 = dpp_add<0x118>(p0); p1 = dpp_add<0x118>(p1); p2 = dpp_add<0x118>(p2);
        if ((lane & 15) == 15) {
            float* dst = lds + L_PART + (3 * q) * 16 + w;
            dst[0] = p0; dst[16] = p1; dst[32] = p2;
        }
    }
    __syncthreads();
    const u32 tag = ((const u32*)lds)[L_TAG];
    if (w == 0) {
        f32x4 pv = *(const f32x4*)(lds + L_PART + lane * 4);
        float z = (lane & 3) == 3 ? 0.f : (pv[0] + pv[1]) + (pv[2] + pv[3]);
        z = dpp_add<0xb1>(z);
        z = dpp_add<0x4e>(z);
        if ((lane & 3) == 0)
            __hip_atomic_store(gran + b * 16 + (lane >> 2), ((u64)tag << 32) | __float_as_uint(z), __ATOMIC_RELAXED, __HIP_MEMORY_SCOPE_AGENT);
    }
    STAMP_AT(2);
    float wuv[R];
    if (w < 3) {
#pragma unroll
        for (int j = 0; j < R; ++j) wuv[j] = bf2f(wul[j * C + t]);
    }
    if (w < 8) {
        const auto rg = __builtin_amdgcn_make_buffer_rsrc((void*)(ws + 1024), 0, NWG * 128, 0x00020000);
        const int off = (32 * w + (lane >> 3)) * 128 + (lane & 7) * 16;
        u32x4 g0 = {0, 0, 0, 0}, g1 = g0, g2 = g0, g3 = g0;
        u64 m0 = 0x3f3f3f3f3f3f3f3full, m1 = m0, m2 = m0, m3 = m0;
        if (PRE_SLEEP) __builtin_amdgcn_s_sleep(PRE_SLEEP);
        for (u32 spin = 0; spin < SPIN_CAP; ++spin) {
            asm volatile(
                "s_mov_b64 exec, %[m0]\n\t"
                "s_cbranch_execz 2f\n\t"
                "buffer_load_dwordx4 %[g0], %[off], %[rg], 0 offen sc1\n"
                "2:\n\t"
                "s_mov_b64 exec, %[m1]\n\t"
                "s_cbranch_execz 3f\n\t"
                "buffer_load_dwordx4 %[g1], %[off], %[rg], 0 offen offset:1024 sc1\n"
                "3:\n\t"
                "s_mov_b64 exec, %[m2]\n\t"
                "s_cbranch_execz 4f\n\t"
                "buffer_load_dwordx4 %[g2], %[off], %[rg], 0 offen offset:2048 sc1\n"
                "4:\n\t"
                "s_mov_b64 exec, %[m3]\n\t"
                "s_cbranch_execz 5f\n\t"
                "buffer_load_dwordx4 %[g3], %[off], %[rg], 0 offen offset:3072 sc1\n"
                "5:\n\t"
                "s_mov_b64 exec, -1\n\t"
                "s_waitcnt vmcnt(0)"
                : [g0] "+v"(g0), [g1] "+v"(g1), [g2] "+v"(g2), [g3] "+v"(g3)
                : [m0] "s"(m0), [m1] "s"(m1), [m2] "s"(m2), [m3] "s"(m3), [off] "v"(off), [rg] "s"(rg)
                : "memory");
            m0 &= __ballot((g0[1] != tag) | (g0[3] != tag));
            m1 &= __ballot((g1[1] != tag) | (g1[3] != tag));
            m2 &= __ballot((g2[1] != tag) | (g2[3] != tag));
            m3 &= __ballot((g3[1] != tag) | (g3[3] != tag));
            if ((m0 | m1 | m2 | m3) == 0) break;
            if (POLL_SLEEP) __builtin_amdgcn_s_sleep(POLL_SLEEP);
        }
        float z0 = (__uint_as_float(g0[0]) + __uint_as_float(g1[0])) + (__uint_as_float(g2[0]) + __uint_as_float(g3[0]));
        float z1 = (__uint_as_float(g0[2]) + __uint_as_float(g1[2])) + (__uint_as_float(g2[2]) + __uint_as_float(g3[2]));
        z0 = dpp_add<0x128>(z0); z1 = dpp_add<0x128>(z1);
        z0 = add_xor32(add_xor16(z0)); z1 = add_xor32(add_xor16(z1));
        if (lane < 6) { lds[L_PART2 + (2 * lane) * 8 + w] = z0; lds[L_PART2 + (2 * lane + 1) * 8 + w] = z1; }
    }
    __syncthreads();
    STAMP_AT(3);
    if (b == 0 && t == 0) __hip_atomic_store(epoch, tag, __ATOMIC_RELAXED, __HIP_MEMORY_SCOPE_AGENT);
    if (w < 3) {
        f32x4 pv = *(const f32x4*)(lds + L_PART2 + (lane & 31) * 4);
        float z = (pv[0] + pv[1]) + (pv[2] + pv[3]);
        z = dpp_add<0xb1>(z);
        z = bfround(z * (1.0f / (512.f * 512.f)));
        const int hb = __float_as_int(bfround(z * __builtin_amdgcn_rcpf(1.f + __expf(-z))));
        float u = 0.f;
#pragma unroll
        for (int j = 0; j < R; ++j) u += __int_as_float(__builtin_amdgcn_readlane(hb, 2 * j)) * wuv[j];
        u = bfround(u);
        lds[L_SV + t] = bfround(__builtin_amdgcn_rcpf(1.f + __expf(-u)));
    }
    __syncthreads();
    {
        const int g = t % 24;
        f32x4 sa = *(const f32x4*)(lds + L_SV + g * 8), sb = *(const f32x4*)(lds + L_SV + g * 8 + 4);
        float t0, t1, t2, t3;
        u32 so;
        STAMP_AT(4);
        asm volatile(
            "s_memrealtime s[60:61]\n\t"
            "s_waitcnt lgkmcnt(0)\n\t"
            "s_add_u32 s62, s60, %[phase]\n\t"
            "s_nop 1\n\t"
            ".set se_i, 0\n\t"
            ".rept 32\n\t"
            "v_lshlrev_b32 %[t0], 16, v[40+4*se_i]\n\t"
            "v_and_b32 %[t1], %[mask], v[40+4*se_i]\n\t"
            "v_lshlrev_b32 %[t2], 16, v[41+4*se_i]\n\t"
            "v_and_b32 %[t3], %[mask], v[41+4*se_i]\n\t"
            "v_mul_f32 %[t0], %[t0], %[s0]\n\t"
            "v_mul_f32 %[t1], %[t1], %[s1]\n\t"
            "v_mul_f32 %[t2], %[t2], %[s2]\n\t"
            "v_mul_f32 %[t3], %[t3], %[s3]\n\t"
            "v_cvt_pk_bf16_f32 v[40+4*se_i], %[t0], %[t1]\n\t"
            "v_cvt_pk_bf16_f32 v[41+4*se_i], %[t2], %[t3]\n\t"
            "v_lshlrev_b32 %[t0], 16, v[42+4*se_i]\n\t"
            "v_and_b32 %[t1], %[mask], v[42+4*se_i]\n\t"
            "v_lshlrev_b32 %[t2], 16, v[43+4*se_i]\n\t"
            "v_and_b32 %[t3], %[mask], v[43+4*se_i]\n\t"
            "v_mul_f32 %[t0], %[t0], %[s4]\n\t"
            "v_mul_f32 %[t1], %[t1], %[s5]\n\t"
            "v_mul_f32 %[t2], %[t2], %[s6]\n\t"
            "v_mul_f32 %[t3], %[t3], %[s7]\n\t"
            "s_mov_b32 %[so], se_i*" STR(STEP) "\n\t"
            "v_cvt_pk_bf16_f32 v[42+4*se_i], %[t0], %[t1]\n\t"
            "v_cvt_pk_bf16_f32 v[43+4*se_i], %[t2], %[t3]\n\t"
            "s_add_u32 s63, s62, (se_i*" PACE_STR ")/32\n"
            "9:\n\t"
            "s_memrealtime s[60:61]\n\t"
            "s_waitcnt lgkmcnt(0)\n\t"
            "s_sub_u32 s61, s60, s63\n\t"
            "s_cmp_lt_i32 s61, 0\n\t"
            "s_cbranch_scc1 9b\n\t"
            "buffer_store_dwordx4 v[40+4*se_i:43+4*se_i], %[voff], %[ro], %[so] offen" ST_POL "\n\t"
            ".set se_i, se_i+1\n\t"
            ".endr\n\t"
            : [t0] "=&v"(t0), [t1] "=&v"(t1), [t2] "=&v"(t2), [t3] "=&v"(t3), [so] "=&s"(so)
            : [s0] "v"(sa[0]), [s1] "v"(sa[1]), [s2] "v"(sa[2]), [s3] "v"(sa[3]),
              [s4] "v"(sb[0]), [s5] "v"(sb[1]), [s6] "v"(sb[2]), [s7] "v"(sb[3]),
              [voff] "v"(voff), [ro] "s"(ro), [mask] "s"(0xffff0000u), [phase] "s"((b & 1) == PWHO ? w * (PACE / 32) / 12 : -100000)
            : DATA_REGS, "s60", "s61", "s62", "s63", "scc", "memory");
    }
}

extern "C" void kernel_launch(void* const* d_in, const int* in_sizes, int n_in,
                              void* d_out, int out_size, void* d_ws, size_t ws_size,
                              hipStream_t stream) {
    se_kernel<<<NWG, NT, 0, stream>>>((const u32x4*)d_in[0], (const u32x4*)d_in[1], (const u32x4*)d_in[2],
                                      (u32x4*)d_out, (u32*)d_ws);
}
```

```cpp
#include <hip/hip_runtime.h>
#include <hip/hip_bf16.h>
#include <stdint.h>

typedef unsigned int u32;
typedef unsigned long long u64;
typedef u32 u32x4 __attribute__((ext_vector_type(4)));
typedef float f32x2 __attribute__((ext_vector_type(2)));
typedef float f32x4 __attribute__((ext_vector_type(4)));
typedef u64 u64x2 __attribute__((ext_vector_type(2)));
typedef __attribute__((address_space(1))) u32 gu32;
typedef __attribute__((address_space(1))) u64 gu64;
#define LAS __attribute__((address_space(3)))

#define C 192
#define R 12
#define NWG 256
#define NT 768
#define STEP 3145728
#define LD_POL " nt"
#define ST_POL " sc0 sc1"
#define SPIN_CAP (1u << 20)
#define LAG 8
#define FIRST 4
#define WIN_E 32
#define PACE 1240
#define STR2(x) #x
#define STR(x) STR2(x)

#define CL10(p) "v" #p "0","v" #p "1","v" #p "2","v" #p "3","v" #p "4","v" #p "5","v" #p "6","v" #p "7","v" #p "8","v" #p "9"
#define DATA_REGS CL10(4), CL10(5), CL10(6), CL10(7), CL10(8), CL10(9), CL10(10), CL10(11), CL10(12), CL10(13), CL10(14), CL10(15), \
    "v160","v161","v162","v163","v164","v165","v166","v167"
#define TMP_REGS "v32","v33","v34","v35","v36","v37","v38","v39"

__device__ __forceinline__ float bf_lo(u32 v) { return __uint_as_float(v << 16); }
__device__ __forceinline__ float bfround(float f) {
    typedef __bf16 bf16x2 __attribute__((ext_vector_type(2)));
    f32x2 v = {f, f};
    return bf_lo(__builtin_bit_cast(u32, __builtin_convertvector(v, bf16x2)));
}
template <int CTRL>
__device__ __forceinline__ float dpp_add(float v) {
    return v + __int_as_float(__builtin_amdgcn_update_dpp(0, __float_as_int(v), CTRL, 0xf, 0xf, true));
}
__device__ __forceinline__ float add_xor16(float v) {
    auto r = __builtin_amdgcn_permlane16_swap(__float_as_uint(v), __float_as_uint(v), false, false);
    return __uint_as_float(r[0]) + __uint_as_float(r[1]);
}
__device__ __forceinline__ float add_xor32(float v) {
    auto r = __builtin_amdgcn_permlane32_swap(__float_as_uint(v), __float_as_uint(v), false, false);
    return __uint_as_float(r[0]) + __uint_as_float(r[1]);
}
template <typename T>
__device__ __forceinline__ LAS T* lds_at(u32 byte) { return (LAS T*)(uintptr_t)byte; }

#define STAMP_AT(k) do { } while (0)

#define RS 448
#define PL (32 * RS)
#define L_RED 0
#define L_PART (2 * PL)
#define L_PART2 (L_PART + 1024)
#define L_SV (L_PART2 + 512)
#define L_W (L_SV + 768)
#define L_END (L_W + NT * 16)

#define UNPACK(K) \
    "v_lshlrev_b32 v32, 16, v[40+4*" K "]\n\t" \
    "v_and_b32 v33, %[mask], v[40+4*" K "]\n\t" \
    "v_lshlrev_b32 v34, 16, v[41+4*" K "]\n\t" \
    "v_and_b32 v35, %[mask], v[41+4*" K "]\n\t" \
    "v_lshlrev_b32 v36, 16, v[42+4*" K "]\n\t" \
    "v_and_b32 v37, %[mask], v[42+4*" K "]\n\t" \
    "v_lshlrev_b32 v38, 16, v[43+4*" K "]\n\t" \
    "v_and_b32 v39, %[mask], v[43+4*" K "]\n\t"
#define ACCUM(K) UNPACK(K) \
    "v_pk_add_f32 v[24:25], v[24:25], v[32:33]\n\t" \
    "v_pk_add_f32 v[26:27], v[26:27], v[34:35]\n\t" \
    "v_pk_add_f32 v[28:29], v[28:29], v[36:37]\n\t" \
    "v_pk_add_f32 v[30:31], v[30:31], v[38:39]\n\t"

__global__ void __launch_bounds__(NT, 3) __attribute__((amdgpu_num_vgpr(20)))
se_kernel(const void* x_, const void* wd_, const void* wu_, void* out_, void* ws_) {
    __shared__ __attribute__((aligned(16))) char lds[L_END];
    u64x2 k01, k23; u64 k4;
    asm volatile("s_load_dwordx4 %0, %3, 0x0\n\ts_load_dwordx4 %1, %3, 0x10\n\ts_load_dwordx2 %2, %3, 0x20\n\ts_waitcnt lgkmcnt(0)"
                 : "=&s"(k01), "=&s"(k23), "=&s"(k4) : "s"(__builtin_amdgcn_kernarg_segment_ptr()));
    const u64 x = k01[0], wd = k01[1], wu = k23[0], out = k23[1], ws = k4;

    const int t = threadIdx.x;
    const int lane = t & 63;
    const int w = __builtin_amdgcn_readfirstlane(t >> 6);
    const int b = blockIdx.x;
    const u32 even_xcc = 1u - (__builtin_amdgcn_s_getreg((3 << 11) | 20) & 1u);
    STAMP_AT(0);

    const u32 reg = (b & 7) * 32 + (b >> 3);
    const u64 base = (u64)reg * (NT * 16);
    const u32 span = 31u * STEP + NT * 16;
    const auto rx = __builtin_amdgcn_make_buffer_rsrc((void*)(x + base), 0, span, 0x00020000);
    const u32 voff = t * 16;

    int t2 = t;
    asm volatile(
        ".set se_i, 0\n\t"
        ".rept " STR(FIRST) "\n\t"
        "s_mov_b32 s63, se_i*" STR(STEP) "\n\t"
        "buffer_load_dwordx4 v[40+4*se_i:43+4*se_i], %[voff], %[rx], s63 offen" LD_POL "\n\t"
        ".set se_i, se_i+1\n\t"
        ".endr"
        : "+v"(t2) : [voff] "v"(voff), [rx] "s"(rx) : DATA_REGS, "s63", "memory");

    const u32 lb = (u32)(uintptr_t)(LAS char*)lds;
    const int lane2 = t2 & 63;
    const int q = lane2 >> 4;
    const u32 slot = 16 * w + (lane2 & 15);
    const u32 pl = slot >= 96 ? 1 : 0, idx = slot - 96 * pl;
    const u32 ch = 8 * (idx >> 2) + 4 * pl + (idx & 3);
    const u64 wp = t2 < 288 ? wu + t2 * 16 : ws;
    const u64 wdp = wd + (ch * R + 3 * q) * 2;
    u32 a_wl = lb + L_W + t2 * 16;
    u32 a_red = lb + L_RED + (t2 / 24) * RS + (t2 % 24) * 16;
    u32 a_rd = lb + L_RED + pl * PL + q * RS + idx * 4;
    u32 a_part = lb + L_PART + (3 * q) * 64 + w * 4;
    u32 a_sv = lb + L_SV + (t2 % 24) * 32;
    if (t2 < 64) *lds_at<float>(lb + L_PART + (t2 >> 2) * 64 + 48 + (t2 & 3) * 4) = 0.f;

    f32x4 accA, accB;
    u32 w0, w1, w2;
    {
        u32x4 wv;
        u32 so;
        asm volatile(
            "s_cmp_lg_u32 %[thr], 0\n\t"
            "global_load_dwordx4 %[wv], %[wp], off\n\t"
            "global_load_ushort %[w0], %[wdp], off\n\t"
            "global_load_ushort %[w1], %[wdp], off offset:2\n\t"
            "global_load_ushort %[w2], %[wdp], off offset:4\n\t"
            "v_mov_b32 v24, 0\n\tv_mov_b32 v25, 0\n\tv_mov_b32 v26, 0\n\tv_mov_b32 v27, 0\n\t"
            "v_mov_b32 v28, 0\n\tv_mov_b32 v29, 0\n\tv_mov_b32 v30, 0\n\tv_mov_b32 v31, 0\n\t"
            ".set se_i, " STR(FIRST) "\n\t"
            ".rept 32-" STR(FIRST) "\n\t"
            ".if se_i >= " STR(WIN_E) "\n\t"
            "s_cbranch_scc0 6f\n\t"
            "s_waitcnt vmcnt(" STR(WIN_E) "-1)\n"
            "6:\n\t"
            ".endif\n\t"
            "s_mov_b32 %[so], se_i*" STR(STEP) "\n\t"
            "buffer_load_dwordx4 v[40+4*se_i:43+4*se_i], %[voff], %[rx], %[so] offen" LD_POL "\n\t"
            ".if se_i >= " STR(LAG) "\n\t"
            ".set se_k, se_i-" STR(LAG) "\n\t"
            "s_waitcnt vmcnt(" STR(LAG) ")\n\t"
            ".if se_k == 4\n\t"
            "ds_write_b128 %[awl], %[wv]\n\t"
            ".endif\n\t"
            ACCUM("se_k")
            ".endif\n\t"
            ".set se_i, se_i+1\n\t"
            ".endr\n\t"
            ".set se_k, 32-" STR(LAG) "\n\t"
            ".rept " STR(LAG) "\n\t"
            "s_waitcnt vmcnt(31-se_k)\n\t"
            ACCUM("se_k")
            ".set se_k, se_k+1\n\t"
            ".endr\n\t"
            "ds_write_b128 %[ared], v[24:27]\n\t"
            "ds_write_b128 %[ared], v[28:31] offset:" STR(PL) "\n\t"
            "s_waitcnt lgkmcnt(0)\n\t"
            "s_barrier"
            : "=&{v[24:27]}"(accA), "=&{v[28:31]}"(accB), [wv] "=&v"(wv), [w0] "=&v"(w0), [w1] "=&v"(w1), [w2] "=&v"(w2), [so] "=&s"(so),
              [ard] "+v"(a_rd), [apart] "+v"(a_part), [asv] "+v"(a_sv)
            : [voff] "v"(voff), [rx] "s"(rx), [wp] "v"(wp), [wdp] "v"(wdp), [awl] "v"(a_wl), [ared] "v"(a_red),
              [mask] "s"(0xffff0000u), [thr] "s"(even_xcc)
            : DATA_REGS, TMP_REGS, "scc", "memory");
    }
    STAMP_AT(1);
    u32 tag = *lds_at<u32>(lb + L_W + 288 * 16) + 1;
    {
        const LAS float* src = lds_at<float>(a_rd);
        constexpr int S = 4 * RS / 4;
        float v0 = src[0], v1 = src[S], v2 = src[2 * S], v3 = src[3 * S], v4 = src[4 * S], v5 = src[5 * S], v6 = src[6 * S], v7 = src[7 * S];
        float sc = ((v0 + v1) + (v2 + v3)) + ((v4 + v5) + (v6 + v7));
        sc = add_xor32(add_xor16(sc));
        float p0 = sc * bf_lo(w0), p1 = sc * bf_lo(w1), p2 = sc * bf_lo(w2);
        p0 = dpp_add<0x111>(p0); p1 = dpp_add<0x111>(p1); p2 = dpp_add<0x111>(p2);
        p0 = dpp_add<0x112>(p0); p1 = dpp_add<0x112>(p1); p2 = dpp_add<0x112>(p2);
        p0 = dpp_add<0x114>(p0); p1 = dpp_add<0x114>(p1); p2 = dpp_add<0x114>(p2);
        p0 = dpp_add<0x118>(p0); p1 = dpp_add<0x118>(p1); p2 = dpp_add<0x118>(p2);
        if ((lane & 15) == 15) {
            LAS float* dst = lds_at<float>(a_part);
            dst[0] = p0; dst[16] = p1; dst[32] = p2;
        }
    }
    const u64 gline = ws + 4096 + (u64)b * 128;
    u32 a_pp = lb + L_PART + (lane & 7) * 128 + (lane >> 3) * 4;
    u32 goff = (lane >> 3) * (NWG * 128) + (lane & 7) * 16;
    asm volatile("" : "+v"(tag), "+v"(a_pp), "+v"(goff));
    {
        asm volatile(
            "s_waitcnt lgkmcnt(0)\n\t"
            "s_barrier\n\t"
            "s_cmp_lg_u32 %[w], 0\n\t"
            "s_cbranch_scc1 1f\n\t"
            "ds_read2_b32 v[24:25], %[app] offset0:0 offset1:8\n\t"
            "ds_read2_b32 v[26:27], %[app] offset0:16 offset1:24\n\t"
            "v_mov_b32 v29, %[tag]\n\t"
            "v_mov_b32 v31, %[tag]\n\t"
            "s_waitcnt lgkmcnt(1)\n\t"
            "v_add_f32 v28, v24, v25\n\t"
            "s_waitcnt lgkmcnt(0)\n\t"
            "v_add_f32 v30, v26, v27\n\t"
            "s_nop 0\n\t"
            "v_add_f32_dpp v28, v28, v28 row_ror:8 row_mask:0xf bank_mask:0xf\n\t"
            "v_add_f32_dpp v30, v30, v30 row_ror:8 row_mask:0xf bank_mask:0xf\n\t"
            "s_nop 0\n\t"
            "v_mov_b32 v24, v28\n\t"
            "v_mov_b32 v25, v30\n\t"
            "s_nop 1\n\t"
            "v_permlane16_swap_b32 v28, v24\n\t"
            "v_permlane16_swap_b32 v30, v25\n\t"
            "s_nop 1\n\t"
            "v_add_f32 v28, v28, v24\n\t"
            "v_add_f32 v30, v30, v25\n\t"
            "v_mov_b32 v24, v28\n\t"
            "v_mov_b32 v25, v30\n\t"
            "s_nop 1\n\t"
            "v_permlane32_swap_b32 v28, v24\n\t"
            "v_permlane32_swap_b32 v30, v25\n\t"
            "s_nop 1\n\t"
            "v_add_f32 v28, v28, v24\n\t"
            "v_add_f32 v30, v30, v25\n\t"
            "global_store_dwordx4 %[goff], v[28:31], %[gline] sc1\n\t"
            "s_nop 1\n"
            "1:"
            :
            : [w] "s"(w), [app] "v"(a_pp), [tag] "v"(tag), [goff] "v"(goff), [gline] "s"(gline)
            : "v24", "v25", "v26", "v27", "v28", "v29", "v30", "v31", "scc", "memory");
    }
    STAMP_AT(2);
    float wuv[R];
    if (w < 3) {
        const LAS unsigned short* wul = lds_at<unsigned short>(lb + L_W + t * 2);
#pragma unroll
        for (int j = 0; j < R; ++j) wuv[j] = bf_lo(wul[j * C]);
    }
    if (w < 8) {
        const auto rg = __builtin_amdgcn_make_buffer_rsrc((void*)(ws + 4096 + (b & 7) * (NWG * 128)), 0, NWG * 128, 0x00020000);
        const u32 off = (32 * w + (lane >> 3)) * 128 + (lane & 7) * 16;
        u32x4 g0, g1, g2, g3;
        u64 m0 = 0x3f3f3f3f3f3f3f3full, m1 = m0, m2 = m0, m3 = m0, ma;
        u32 spin = SPIN_CAP;
        asm volatile(
            "s_sleep 4\n"
            "0:\n\t"
            "s_mov_b64 exec, %[m0]\n\t"
            "s_cbranch_execz 2f\n\t"
            "buffer_load_dwordx4 v[24:27], %[off], %[rg], 0 offen sc1\n"
            "2:\n\t"
            "s_mov_b64 exec, %[m1]\n\t"
            "s_cbranch_execz 3f\n\t"
            "buffer_load_dwordx4 v[28:31], %[off], %[rg], 0 offen offset:1024 sc1\n"
            "3:\n\t"
            "s_mov_b64 exec, %[m2]\n\t"
            "s_cbranch_execz 4f\n\t"
            "buffer_load_dwordx4 v[32:35], %[off], %[rg], 0 offen offset:2048 sc1\n"
            "4:\n\t"
            "s_mov_b64 exec, %[m3]\n\t"
            "s_cbranch_execz 5f\n\t"
            "buffer_load_dwordx4 v[36:39], %[off], %[rg], 0 offen offset:3072 sc1\n"
            "5:\n\t"
            "s_mov_b64 exec, -1\n\t"
            "s_waitcnt vmcnt(0)\n\t"
            "v_cmp_ne_u32 vcc, %[tag], v25\n\t"
            "v_cmp_ne_u32 %[ma], %[tag], v27\n\t"
            "s_or_b64 vcc, vcc, %[ma]\n\t"
            "s_and_b64 %[m0], %[m0], vcc\n\t"
            "v_cmp_ne_u32 vcc, %[tag], v29\n\t"
            "v_cmp_ne_u32 %[ma], %[tag], v31\n\t"
            "s_or_b64 vcc, vcc, %[ma]\n\t"
            "s_and_b64 %[m1], %[m1], vcc\n\t"
            "v_cmp_ne_u32 vcc, %[tag], v33\n\t"
            "v_cmp_ne_u32 %[ma], %[tag], v35\n\t"
            "s_or_b64 vcc, vcc, %[ma]\n\t"
            "s_and_b64 %[m2], %[m2], vcc\n\t"
            "v_cmp_ne_u32 vcc, %[tag], v37\n\t"
            "v_cmp_ne_u32 %[ma], %[tag], v39\n\t"
            "s_or_b64 vcc, vcc, %[ma]\n\t"
            "s_and_b64 %[m3], %[m3], vcc\n\t"
            "s_or_b64 %[ma], %[m0], %[m1]\n\t"
            "s_or_b64 vcc, %[m2], %[m3]\n\t"
            "s_or_b64 %[ma], %[ma], vcc\n\t"
            "s_cbranch_scc0 7f\n\t"
            "s_sub_u32 %[spin], %[spin], 1\n\t"
            "s_cbranch_scc0 0b\n"
            "7:"
            : "=&{v[24:27]}"(g0), "=&{v[28:31]}"(g1), "=&{v[32:35]}"(g2), "=&{v[36:39]}"(g3),
              [m0] "+s"(m0), [m1] "+s"(m1), [m2] "+s"(m2), [m3] "+s"(m3), [ma] "=&s"(ma), [spin] "+s"(spin)
            : [off] "v"(off), [rg] "s"(rg), [tag] "s"(__builtin_amdgcn_readfirstlane(tag))
            : "vcc", "scc", "memory");
        float z0 = (__uint_as_float(g0[0]) + __uint_as_float(g1[0])) + (__uint_as_float(g2[0]) + __uint_as_float(g3[0]));
        float z1 = (__uint_as_float(g0[2]) + __uint_as_float(g1[2])) + (__uint_as_float(g2[2]) + __uint_as_float(g3[2]));
        z0 = dpp_add<0x128>(z0); z1 = dpp_add<0x128>(z1);
        z0 = add_xor32(add_xor16(z0)); z1 = add_xor32(add_xor16(z1));
        if (lane < 6) { LAS float* d = lds_at<float>(lb + L_PART2 + lane * 64 + w * 4); d[0] = z0; d[8] = z1; }
    }
    __syncthreads();
    STAMP_AT(3);
    if (b == 0 && t == 0) __hip_atomic_store((gu32*)ws, tag, __ATOMIC_RELAXED, __HIP_MEMORY_SCOPE_AGENT);
    if (w < 3) {
        f32x4 pv = *lds_at<f32x4>(lb + L_PART2 + (lane & 31) * 16);
        float z = (pv[0] + pv[1]) + (pv[2] + pv[3]);
        z = dpp_add<0xb1>(z);
        z = bfround(z * (1.0f / (512.f * 512.f)));
        const int hb = __float_as_int(bfround(z * __builtin_amdgcn_rcpf(1.f + __expf(-z))));
        float ua = 0.f, ub = 0.f, uc = 0.f;
#pragma unroll
        for (int j = 0; j < 4; ++j) {
            ua += __int_as_float(__builtin_amdgcn_readlane(hb, 2 * j)) * wuv[j];
            ub += __int_as_float(__builtin_amdgcn_readlane(hb, 2 * (j + 4))) * wuv[j + 4];
            uc += __int_as_float(__builtin_amdgcn_readlane(hb, 2 * (j + 8))) * wuv[j + 8];
        }
        const float u = bfround((ua + ub) + uc);
        *lds_at<float>(lb + L_SV + t * 4) = bfround(__builtin_amdgcn_rcpf(1.f + __expf(-u)));
    }
    __syncthreads();
    {
        const f32x4 sa = *lds_at<f32x4>(a_sv), sb = *lds_at<f32x4>(a_sv + 16);
        const auto ro = __builtin_amdgcn_make_buffer_rsrc((void*)(out + base), 0, span, 0x00020000);
        u32 so;
        STAMP_AT(4);
        asm volatile(
            "s_memrealtime s[60:61]\n\t"
            "s_waitcnt lgkmcnt(0)\n\t"
            "s_add_u32 s62, s60, %[phase]\n\t"
            ".set se_i, 0\n\t"
            ".rept 32\n\t"
            UNPACK("se_i")
            "v_pk_mul_f32 v[32:33], v[32:33], v[24:25]\n\t"
            "v_pk_mul_f32 v[34:35], v[34:35], v[26:27]\n\t"
            "v_pk_mul_f32 v[36:37], v[36:37], v[28:29]\n\t"
            "v_pk_mul_f32 v[38:39], v[38:39], v[30:31]\n\t"
            "s_mov_b32 %[so], se_i*" STR(STEP) "\n\t"
            "v_cvt_pk_bf16_f32 v[40+4*se_i], v32, v33\n\t"
            "v_cvt_pk_bf16_f32 v[41+4*se_i], v34, v35\n\t"
            "v_cvt_pk_bf16_f32 v[42+4*se_i], v36, v37\n\t"
            "v_cvt_pk_bf16_f32 v[43+4*se_i], v38, v39\n\t"
            "s_add_u32 s63, s62, (se_i*" STR(PACE) ")/32\n"
            "9:\n\t"
            "s_memrealtime s[60:61]\n\t"
            "s_waitcnt lgkmcnt(0)\n\t"
            "s_sub_u32 s61, s60, s63\n\t"
            "s_cmp_lt_i32 s61, 0\n\t"
            "s_cbranch_scc1 9b\n\t"
            "buffer_store_dwordx4 v[40+4*se_i:43+4*se_i], %[voff], %[ro], %[so] offen" ST_POL "\n\t"
            ".set se_i, se_i+1\n\t"
            ".endr\n\t"
            : [so] "=&s"(so)
            : "{v[24:27]}"(sa), "{v[28:31]}"(sb), [voff] "v"(voff), [ro] "s"(ro), [mask] "s"(0xffff0000u),
              [phase] "s"(even_xcc ? (u32)(w * (PACE / 32) / 12) : (u32)-100000)
            : DATA_REGS, TMP_REGS, "s60", "s61", "s62", "s63", "scc", "memory");
    }
}

extern "C" void kernel_launch(void* const* d_in, const int* in_sizes, int n_in,
                              void* d_out, int out_size, void* d_ws, size_t ws_size,
                              hipStream_t stream) {
    se_kernel<<<NWG, NT, 0, stream>>>(d_in[0], d_in[1], d_in[2], d_out, d_ws);
}
```

```cpp
#define PROBE_A 0
#define PROBE_B 0
#define PROBE_WG 0
#define PROBE_M 8
#define PROBE_W 11
#include <hip/hip_runtime.h>
#include <hip/hip_bf16.h>
#include <stdint.h>

typedef unsigned int u32;
typedef unsigned long long u64;
typedef u32 u32x4 __attribute__((ext_vector_type(4)));
typedef float f32x2 __attribute__((ext_vector_type(2)));
typedef float f32x4 __attribute__((ext_vector_type(4)));
typedef u64 u64x2 __attribute__((ext_vector_type(2)));
typedef __attribute__((address_space(1))) u32 gu32;
typedef __attribute__((address_space(1))) u64 gu64;
#define LAS __attribute__((address_space(3)))

#define C 192
#define R 12
#define NWG 256
#define NT 768
#define STEP 3145728
#define LD_POL " nt"
#define ST_POL " sc0 sc1"
#define SPIN_CAP (1u << 20)
#define LAG 8
#define FIRST 4
#define WIN_E 32
#define PACE 1240
#define STR2(x) #x
#define STR(x) STR2(x)

#define CL10(p) "v" #p "0","v" #p "1","v" #p "2","v" #p "3","v" #p "4","v" #p "5","v" #p "6","v" #p "7","v" #p "8","v" #p "9"
#define DATA_REGS CL10(4), CL10(5), CL10(6), CL10(7), CL10(8), CL10(9), CL10(10), CL10(11), CL10(12), CL10(13), CL10(14), CL10(15), \
    "v160","v161","v162","v163","v164","v165","v166","v167"
#define TMP_REGS "v32","v33","v34","v35","v36","v37","v38","v39"

__device__ __forceinline__ float bf_lo(u32 v) { return __uint_as_float(v << 16); }
__device__ __forceinline__ float bfround(float f) {
    typedef __bf16 bf16x2 __attribute__((ext_vector_type(2)));
    f32x2 v = {f, f};
    return bf_lo(__builtin_bit_cast(u32, __builtin_convertvector(v, bf16x2)));
}
template <int CTRL>
__device__ __forceinline__ float dpp_add(float v) {
    return v + __int_as_float(__builtin_amdgcn_update_dpp(0, __float_as_int(v), CTRL, 0xf, 0xf, true));
}
__device__ __forceinline__ float add_xor16(float v) {
    auto r = __builtin_amdgcn_permlane16_swap(__float_as_uint(v), __float_as_uint(v), false, false);
    return __uint_as_float(r[0]) + __uint_as_float(r[1]);
}
__device__ __forceinline__ float add_xor32(float v) {
    auto r = __builtin_amdgcn_permlane32_swap(__float_as_uint(v), __float_as_uint(v), false, false);
    return __uint_as_float(r[0]) + __uint_as_float(r[1]);
}
template <typename T>
__device__ __forceinline__ LAS T* lds_at(u32 byte) { return (LAS T*)(uintptr_t)byte; }

#define STAMP_AT(k) do { ts[k] = __builtin_amdgcn_s_memrealtime(); } while (0)

#define RS 448
#define PL (32 * RS)
#define L_RED 0
#define L_PART (2 * PL)
#define L_PART2 (L_PART + 1024)
#define L_SV (L_PART2 + 512)
#define L_W (L_SV + 768)
#define L_END (L_W + NT * 16)

#define UNPACK(K) \
    "v_lshlrev_b32 v32, 16, v[40+4*" K "]\n\t" \
    "v_and_b32 v33, %[mask], v[40+4*" K "]\n\t" \
    "v_lshlrev_b32 v34, 16, v[41+4*" K "]\n\t" \
    "v_and_b32 v35, %[mask], v[41+4*" K "]\n\t" \
    "v_lshlrev_b32 v36, 16, v[42+4*" K "]\n\t" \
    "v_and_b32 v37, %[mask], v[42+4*" K "]\n\t" \
    "v_lshlrev_b32 v38, 16, v[43+4*" K "]\n\t" \
    "v_and_b32 v39, %[mask], v[43+4*" K "]\n\t"
#define ACCUM(K) UNPACK(K) \
    "v_pk_add_f32 v[24:25], v[24:25], v[32:33]\n\t" \
    "v_pk_add_f32 v[26:27], v[26:27], v[34:35]\n\t" \
    "v_pk_add_f32 v[28:29], v[28:29], v[36:37]\n\t" \
    "v_pk_add_f32 v[30:31], v[30:31], v[38:39]\n\t"

__global__ void __launch_bounds__(NT, 3) __attribute__((amdgpu_num_vgpr(20)))
se_kernel(const void* x_, const void* wd_, const void* wu_, void* out_, void* ws_) {
    __shared__ __attribute__((aligned(16))) char lds[L_END];
    u64x2 k01, k23; u64 k4;
    u64 t_entry;
    asm volatile("s_memrealtime %3\n\ts_waitcnt lgkmcnt(0)\n\ts_load_dwordx4 %0, %4, 0x0\n\ts_load_dwordx4 %1, %4, 0x10\n\ts_load_dwordx2 %2, %4, 0x20\n\ts_waitcnt lgkmcnt(0)"
                 : "=&s"(k01), "=&s"(k23), "=&s"(k4), "=&s"(t_entry) : "s"(__builtin_amdgcn_kernarg_segment_ptr()));
    const u64 x = k01[0], wd = k01[1], wu = k23[0], out = k23[1], ws = k4;

    const int t = threadIdx.x;
    const int lane = t & 63;
    const int w = __builtin_amdgcn_readfirstlane(t >> 6);
    const int b = blockIdx.x;
    const u32 even_xcc = 1u - (__builtin_amdgcn_s_getreg((3 << 11) | 20) & 1u);
    u64 ts[8];
    STAMP_AT(0);
    ts[7] = t_entry;

    const u32 reg = (b & 7) * 32 + (b >> 3);
    const u64 base = (u64)reg * (NT * 16);
    const u32 span = 31u * STEP + NT * 16;
    const auto rx = __builtin_amdgcn_make_buffer_rsrc((void*)(x + base), 0, span, 0x00020000);
    const u32 voff = t * 16;

    int t2 = t;
    asm volatile(
        ".set se_i, 0\n\t"
        ".rept " STR(FIRST) "\n\t"
        "s_mov_b32 s63, se_i*" STR(STEP) "\n\t"
        "buffer_load_dwordx4 v[40+4*se_i:43+4*se_i], %[voff], %[rx], s63 offen" LD_POL "\n\t"
        ".set se_i, se_i+1\n\t"
        ".endr"
        : "+v"(t2) : [voff] "v"(voff), [rx] "s"(rx) : DATA_REGS, "s63", "memory");

    const u32 lb = (u32)(uintptr_t)(LAS char*)lds;
    const int lane2 = t2 & 63;
    const int q = lane2 >> 4;
    const u32 slot = 16 * w + (lane2 & 15);
    const u32 pl = slot >= 96 ? 1 : 0, idx = slot - 96 * pl;
    const u32 ch = 8 * (idx >> 2) + 4 * pl + (idx & 3);
    const u64 wp = t2 < 288 ? wu + t2 * 16 : ws;
    const u64 wdp = wd + (ch * R + 3 * q) * 2;
    u32 a_wl = lb + L_W + t2 * 16;
    u32 a_red = lb + L_RED + (t2 / 24) * RS + (t2 % 24) * 16;
    u32 a_rd = lb + L_RED + pl * PL + q * RS + idx * 4;
    u32 a_part = lb + L_PART + (3 * q) * 64 + w * 4;
    u32 a_sv = lb + L_SV + (t2 % 24) * 32;
    if (t2 < 64) *lds_at<float>(lb + L_PART + (t2 >> 2) * 64 + 48 + (t2 & 3) * 4) = 0.f;

    f32x4 accA, accB;
    u32 w0, w1, w2;
    {
        u32x4 wv;
        u32 so;
        asm volatile(
            "s_cmp_lg_u32 %[thr], 0\n\t"
            "global_load_dwordx4 %[wv], %[wp], off\n\t"
            "global_load_ushort %[w0], %[wdp], off\n\t"
            "global_load_ushort %[w1], %[wdp], off offset:2\n\t"
            "global_load_ushort %[w2], %[wdp], off offset:4\n\t"
            "v_mov_b32 v24, 0\n\tv_mov_b32 v25, 0\n\tv_mov_b32 v26, 0\n\tv_mov_b32 v27, 0\n\t"
            "v_mov_b32 v28, 0\n\tv_mov_b32 v29, 0\n\tv_mov_b32 v30, 0\n\tv_mov_b32 v31, 0\n\t"
            ".set se_i, " STR(FIRST) "\n\t"
            ".rept 32-" STR(FIRST) "\n\t"
            ".if se_i >= " STR(WIN_E) "\n\t"
            "s_cbranch_scc0 6f\n\t"
            "s_waitcnt vmcnt(" STR(WIN_E) "-1)\n"
            "6:\n\t"
            ".endif\n\t"
            "s_mov_b32 %[so], se_i*" STR(STEP) "\n\t"
            "buffer_load_dwordx4 v[40+4*se_i:43+4*se_i], %[voff], %[rx], %[so] offen" LD_POL "\n\t"
            ".if se_i >= " STR(LAG) "\n\t"
            ".set se_k, se_i-" STR(LAG) "\n\t"
            "s_waitcnt vmcnt(" STR(LAG) ")\n\t"
            ".if se_k == 4\n\t"
            "ds_write_b128 %[awl], %[wv]\n\t"
            ".endif\n\t"
            ACCUM("se_k")
            ".endif\n\t"
            ".set se_i, se_i+1\n\t"
            ".endr\n\t"
            ".set se_k, 32-" STR(LAG) "\n\t"
            ".rept " STR(LAG) "\n\t"
            "s_waitcnt vmcnt(31-se_k)\n\t"
            ACCUM("se_k")
            ".set se_k, se_k+1\n\t"
            ".endr\n\t"
            "ds_write_b128 %[ared], v[24:27]\n\t"
            "ds_write_b128 %[ared], v[28:31] offset:" STR(PL) "\n\t"
            "s_waitcnt lgkmcnt(0)\n\t"
            "s_barrier"
            : "=&{v[24:27]}"(accA), "=&{v[28:31]}"(accB), [wv] "=&v"(wv), [w0] "=&v"(w0), [w1] "=&v"(w1), [w2] "=&v"(w2), [so] "=&s"(so),
              [ard] "+v"(a_rd), [apart] "+v"(a_part), [asv] "+v"(a_sv)
            : [voff] "v"(voff), [rx] "s"(rx), [wp] "v"(wp), [wdp] "v"(wdp), [awl] "v"(a_wl), [ared] "v"(a_red),
              [mask] "s"(0xffff0000u), [thr] "s"(even_xcc)
            : DATA_REGS, TMP_REGS, "scc", "memory");
    }
    STAMP_AT(1);
    u32 tag = *lds_at<u32>(lb + L_W + 288 * 16) + 1;
    {
        const LAS float* src = lds_at<float>(a_rd);
        constexpr int S = 4 * RS / 4;
        float v0 = src[0], v1 = src[S], v2 = src[2 * S], v3 = src[3 * S], v4 = src[4 * S], v5 = src[5 * S], v6 = src[6 * S], v7 = src[7 * S];
        float sc = ((v0 + v1) + (v2 + v3)) + ((v4 + v5) + (v6 + v7));
        sc = add_xor32(add_xor16(sc));
        float p0 = sc * bf_lo(w0), p1 = sc * bf_lo(w1), p2 = sc * bf_lo(w2);
        p0 = dpp_add<0x111>(p0); p1 = dpp_add<0x111>(p1); p2 = dpp_add<0x111>(p2);
        p0 = dpp_add<0x112>(p0); p1 = dpp_add<0x112>(p1); p2 = dpp_add<0x112>(p2);
        p0 = dpp_add<0x114>(p0); p1 = dpp_add<0x114>(p1); p2 = dpp_add<0x114>(p2);
        p0 = dpp_add<0x118>(p0); p1 = dpp_add<0x118>(p1); p2 = dpp_add<0x118>(p2);
        if ((lane & 15) == 15) {
            LAS float* dst = lds_at<float>(a_part);
            dst[0] = p0; dst[16] = p1; dst[32] = p2;
        }
    }
    const u64 gline = ws + 4096 + (u64)b * 128;
    u32 a_pp = lb + L_PART + (lane & 7) * 128 + (lane >> 3) * 4;
    u32 goff = (lane >> 3) * (NWG * 128) + (lane & 7) * 16;
    asm volatile("" : "+v"(tag), "+v"(a_pp), "+v"(goff));
    {
        asm volatile(
            "s_waitcnt lgkmcnt(0)\n\t"
            "s_barrier\n\t"
            "s_cmp_lg_u32 %[w], 0\n\t"
            "s_cbranch_scc1 1f\n\t"
            "ds_read2_b32 v[24:25], %[app] offset0:0 offset1:8\n\t"
            "ds_read2_b32 v[26:27], %[app] offset0:16 offset1:24\n\t"
            "v_mov_b32 v29, %[tag]\n\t"
            "v_mov_b32 v31, %[tag]\n\t"
            "s_waitcnt lgkmcnt(1)\n\t"
            "v_add_f32 v28, v24, v25\n\t"
            "s_waitcnt lgkmcnt(0)\n\t"
            "v_add_f32 v30, v26, v27\n\t"
            "s_nop 0\n\t"
            "v_add_f32_dpp v28, v28, v28 row_ror:8 row_mask:0xf bank_mask:0xf\n\t"
            "v_add_f32_dpp v30, v30, v30 row_ror:8 row_mask:0xf bank_mask:0xf\n\t"
            "s_nop 0\n\t"
            "v_mov_b32 v24, v28\n\t"
            "v_mov_b32 v25, v30\n\t"
            "s_nop 1\n\t"
            "v_permlane16_swap_b32 v28, v24\n\t"
            "v_permlane16_swap_b32 v30, v25\n\t"
            "s_nop 1\n\t"
            "v_add_f32 v28, v28, v24\n\t"
            "v_add_f32 v30, v30, v25\n\t"
            "v_mov_b32 v24, v28\n\t"
            "v_mov_b32 v25, v30\n\t"
            "s_nop 1\n\t"
            "v_permlane32_swap_b32 v28, v24\n\t"
            "v_permlane32_swap_b32 v30, v25\n\t"
            "s_nop 1\n\t"
            "v_add_f32 v28, v28, v24\n\t"
            "v_add_f32 v30, v30, v25\n\t"
            "global_store_dwordx4 %[goff], v[28:31], %[gline] sc1\n\t"
            "s_nop 1\n"
            "1:"
            :
            : [w] "s"(w), [app] "v"(a_pp), [tag] "v"(tag), [goff] "v"(goff), [gline] "s"(gline)
            : "v24", "v25", "v26", "v27", "v28", "v29", "v30", "v31", "scc", "memory");
    }
    STAMP_AT(2);
    float wuv[R];
    if (w < 3) {
        const LAS unsigned short* wul = lds_at<unsigned short>(lb + L_W + t * 2);
#pragma unroll
        for (int j = 0; j < R; ++j) wuv[j] = bf_lo(wul[j * C]);
    }
    if (w < 8) {
        const auto rg = __builtin_amdgcn_make_buffer_rsrc((void*)(ws + 4096 + (b & 7) * (NWG * 128)), 0, NWG * 128, 0x00020000);
        const u32 off = (32 * w + (lane >> 3)) * 128 + (lane & 7) * 16;
        u32x4 g0, g1, g2, g3;
        u64 m0 = 0x3f3f3f3f3f3f3f3full, m1 = m0, m2 = m0, m3 = m0, ma;
        u32 spin = SPIN_CAP;
        asm volatile(
            "s_sleep 4\n"
            "0:\n\t"
            "s_mov_b64 exec, %[m0]\n\t"
            "s_cbranch_execz 2f\n\t"
            "buffer_load_dwordx4 v[24:27], %[off], %[rg], 0 offen sc1\n"
            "2:\n\t"
            "s_mov_b64 exec, %[m1]\n\t"
            "s_cbranch_execz 3f\n\t"
            "buffer_load_dwordx4 v[28:31], %[off], %[rg], 0 offen offset:1024 sc1\n"
            "3:\n\t"
            "s_mov_b64 exec, %[m2]\n\t"
            "s_cbranch_execz 4f\n\t"
            "buffer_load_dwordx4 v[32:35], %[off], %[rg], 0 offen offset:2048 sc1\n"
            "4:\n\t"
            "s_mov_b64 exec, %[m3]\n\t"
            "s_cbranch_execz 5f\n\t"
            "buffer_load_dwordx4 v[36:39], %[off], %[rg], 0 offen offset:3072 sc1\n"
            "5:\n\t"
            "s_mov_b64 exec, -1\n\t"
            "s_waitcnt vmcnt(0)\n\t"
            "v_cmp_ne_u32 vcc, %[tag], v25\n\t"
            "v_cmp_ne_u32 %[ma], %[tag], v27\n\t"
            "s_or_b64 vcc, vcc, %[ma]\n\t"
            "s_and_b64 %[m0], %[m0], vcc\n\t"
            "v_cmp_ne_u32 vcc, %[tag], v29\n\t"
            "v_cmp_ne_u32 %[ma], %[tag], v31\n\t"
            "s_or_b64 vcc, vcc, %[ma]\n\t"
            "s_and_b64 %[m1], %[m1], vcc\n\t"
            "v_cmp_ne_u32 vcc, %[tag], v33\n\t"
            "v_cmp_ne_u32 %[ma], %[tag], v35\n\t"
            "s_or_b64 vcc, vcc, %[ma]\n\t"
            "s_and_b64 %[m2], %[m2], vcc\n\t"
            "v_cmp_ne_u32 vcc, %[tag], v37\n\t"
            "v_cmp_ne_u32 %[ma], %[tag], v39\n\t"
            "s_or_b64 vcc, vcc, %[ma]\n\t"
            "s_and_b64 %[m3], %[m3], vcc\n\t"
            "s_or_b64 %[ma], %[m0], %[m1]\n\t"
            "s_or_b64 vcc, %[m2], %[m3]\n\t"
            "s_or_b64 %[ma], %[ma], vcc\n\t"
            "s_cbranch_scc0 7f\n\t"
            "s_sub_u32 %[spin], %[spin], 1\n\t"
            "s_cbranch_scc0 0b\n"
            "7:"
            : "=&{v[24:27]}"(g0), "=&{v[28:31]}"(g1), "=&{v[32:35]}"(g2), "=&{v[36:39]}"(g3),
              [m0] "+s"(m0), [m1] "+s"(m1), [m2] "+s"(m2), [m3] "+s"(m3), [ma] "=&s"(ma), [spin] "+s"(spin)
            : [off] "v"(off), [rg] "s"(rg), [tag] "s"(__builtin_amdgcn_readfirstlane(tag))
            : "vcc", "scc", "memory");
        float z0 = (__uint_as_float(g0[0]) + __uint_as_float(g1[0])) + (__uint_as_float(g2[0]) + __uint_as_float(g3[0]));
        float z1 = (__uint_as_float(g0[2]) + __uint_as_float(g1[2])) + (__uint_as_float(g2[2]) + __uint_as_float(g3[2]));
        z0 = dpp_add<0x128>(z0); z1 = dpp_add<0x128>(z1);
        z0 = add_xor32(add_xor16(z0)); z1 = add_xor32(add_xor16(z1));
        if (lane < 6) { LAS float* d = lds_at<float>(lb + L_PART2 + lane * 64 + w * 4); d[0] = z0; d[8] = z1; }
    }
    __syncthreads();
    STAMP_AT(3);
    if (b == 0 && t == 0) __hip_atomic_store((gu32*)ws, tag, __ATOMIC_RELAXED, __HIP_MEMORY_SCOPE_AGENT);
    if (w < 3) {
        f32x4 pv = *lds_at<f32x4>(lb + L_PART2 + (lane & 31) * 16);
        float z = (pv[0] + pv[1]) + (pv[2] + pv[3]);
        z = dpp_add<0xb1>(z);
        z = bfround(z * (1.0f / (512.f * 512.f)));
        const int hb = __float_as_int(bfround(z * __builtin_amdgcn_rcpf(1.f + __expf(-z))));
        float ua = 0.f, ub = 0.f, uc = 0.f;
#pragma unroll
        for (int j = 0; j < 4; ++j) {
            ua += __int_as_float(__builtin_amdgcn_readlane(hb, 2 * j)) * wuv[j];
            ub += __int_as_float(__builtin_amdgcn_readlane(hb, 2 * (j + 4))) * wuv[j + 4];
            uc += __int_as_float(__builtin_amdgcn_readlane(hb, 2 * (j + 8))) * wuv[j + 8];
        }
        const float u = bfround((ua + ub) + uc);
        *lds_at<float>(lb + L_SV + t * 4) = bfround(__builtin_amdgcn_rcpf(1.f + __expf(-u)));
    }
    __syncthreads();
    {
        const f32x4 sa = *lds_at<f32x4>(a_sv), sb = *lds_at<f32x4>(a_sv + 16);
        const auto ro = __builtin_amdgcn_make_buffer_rsrc((void*)(out + base), 0, span, 0x00020000);
        u32 so;
        STAMP_AT(4);
        asm volatile(
            "s_memrealtime s[60:61]\n\t"
            "s_waitcnt lgkmcnt(0)\n\t"
            "s_add_u32 s62, s60, %[phase]\n\t"
            ".set se_i, 0\n\t"
            ".rept 32\n\t"
            UNPACK("se_i")
            "v_pk_mul_f32 v[32:33], v[32:33], v[24:25]\n\t"
            "v_pk_mul_f32 v[34:35], v[34:35], v[26:27]\n\t"
            "v_pk_mul_f32 v[36:37], v[36:37], v[28:29]\n\t"
            "v_pk_mul_f32 v[38:39], v[38:39], v[30:31]\n\t"
            "s_mov_b32 %[so], se_i*" STR(STEP) "\n\t"
            "v_cvt_pk_bf16_f32 v[40+4*se_i], v32, v33\n\t"
            "v_cvt_pk_bf16_f32 v[41+4*se_i], v34, v35\n\t"
            "v_cvt_pk_bf16_f32 v[42+4*se_i], v36, v37\n\t"
            "v_cvt_pk_bf16_f32 v[43+4*se_i], v38, v39\n\t"
            "s_add_u32 s63, s62, (se_i*" STR(PACE) ")/32\n"
            "9:\n\t"
            "s_memrealtime s[60:61]\n\t"
            "s_waitcnt lgkmcnt(0)\n\t"
            "s_sub_u32 s61, s60, s63\n\t"
            "s_cmp_lt_i32 s61, 0\n\t"
            "s_cbranch_scc1 9b\n\t"
            "buffer_store_dwordx4 v[40+4*se_i:43+4*se_i], %[voff], %[ro], %[so] offen" ST_POL "\n\t"
            ".set se_i, se_i+1\n\t"
            ".endr\n\t"
            : [so] "=&s"(so)
            : "{v[24:27]}"(sa), "{v[28:31]}"(sb), [voff] "v"(voff), [ro] "s"(ro), [mask] "s"(0xffff0000u),
              [phase] "s"(even_xcc ? (u32)(w * (PACE / 32) / 12) : (u32)-100000)
            : DATA_REGS, TMP_REGS, "s60", "s61", "s62", "s63", "scc", "memory");
    }
    STAMP_AT(5);
    asm volatile("s_waitcnt vmcnt(0)" ::: "memory");
    STAMP_AT(6);
    if (b == PROBE_WG && w == PROBE_W) {
        const u64 d = (ts[PROBE_B] - ts[PROBE_A]) * PROBE_M;
        const u64 t1 = __builtin_amdgcn_s_memrealtime();
        while (__builtin_amdgcn_s_memrealtime() - t1 < d) __builtin_amdgcn_s_sleep(2);
    }
}

extern "C" void kernel_launch(void* const* d_in, const int* in_sizes, int n_in,
                              void* d_out, int out_size, void* d_ws, size_t ws_size,
                              hipStream_t stream) {
    se_kernel<<<NWG, NT, 0, stream>>>(d_in[0], d_in[1], d_in[2], d_out, d_ws);
}
```

```cpp
#include <hip/hip_runtime.h>
#include <hip/hip_bf16.h>
#include <stdint.h>

typedef unsigned int u32;
typedef unsigned long long u64;
typedef u32 u32x4 __attribute__((ext_vector_type(4)));
typedef float f32x2 __attribute__((ext_vector_type(2)));
typedef float f32x4 __attribute__((ext_vector_type(4)));
typedef u64 u64x2 __attribute__((ext_vector_type(2)));
typedef __attribute__((address_space(1))) u32 gu32;
typedef __attribute__((address_space(1))) u64 gu64;
#define LAS __attribute__((address_space(3)))

#define C 192
#define R 12
#define NWG 256
#define NT 768
#define STEP 3145728
#define LD_POL " nt"
#define ST_POL " sc0 sc1"
#define SPIN_CAP (1u << 20)
#define LAG 8
#define FIRST 4
#define WARM_AT 20
#define WARM_WAVE 11
#define WARM_POL " sc1"
#define WIN_E 32
#define PACE 1240
#define STR2(x) #x
#define STR(x) STR2(x)

#define CL10(p) "v" #p "0","v" #p "1","v" #p "2","v" #p "3","v" #p "4","v" #p "5","v" #p "6","v" #p "7","v" #p "8","v" #p "9"
#define DATA_REGS CL10(4), CL10(5), CL10(6), CL10(7), CL10(8), CL10(9), CL10(10), CL10(11), CL10(12), CL10(13), CL10(14), CL10(15), \
    "v160","v161","v162","v163","v164","v165","v166","v167"
#define TMP_REGS "v32","v33","v34","v35","v36","v37","v38","v39"

__device__ __forceinline__ float bf_lo(u32 v) { return __uint_as_float(v << 16); }
__device__ __forceinline__ float bfround(float f) {
    typedef __bf16 bf16x2 __attribute__((ext_vector_type(2)));
    f32x2 v = {f, f};
    return bf_lo(__builtin_bit_cast(u32, __builtin_convertvector(v, bf16x2)));
}
template <int CTRL>
__device__ __forceinline__ float dpp_add(float v) {
    return v + __int_as_float(__builtin_amdgcn_update_dpp(0, __float_as_int(v), CTRL, 0xf, 0xf, true));
}
__device__ __forceinline__ float add_xor16(float v) {
    auto r = __builtin_amdgcn_permlane16_swap(__float_as_uint(v), __float_as_uint(v), false, false);
    return __uint_as_float(r[0]) + __uint_as_float(r[1]);
}
__device__ __forceinline__ float add_xor32(float v) {
    auto r = __builtin_amdgcn_permlane32_swap(__float_as_uint(v), __float_as_uint(v), false, false);
    return __uint_as_float(r[0]) + __uint_as_float(r[1]);
}
template <typename T>
__device__ __forceinline__ LAS T* lds_at(u32 byte) { return (LAS T*)(uintptr_t)byte; }

#define STAMP_AT(k) do { } while (0)

#define RS 448
#define PL (32 * RS)
#define L_RED 0
#define L_PART (2 * PL)
#define L_PART2 (L_PART + 1024)
#define L_SV (L_PART2 + 512)
#define L_W (L_SV + 768)
#define L_END (L_W + NT * 16)

#define UNPACK(K) \
    "v_lshlrev_b32 v32, 16, v[40+4*" K "]\n\t" \
    "v_and_b32 v33, %[mask], v[40+4*" K "]\n\t" \
    "v_lshlrev_b32 v34, 16, v[41+4*" K "]\n\t" \
    "v_and_b32 v35, %[mask], v[41+4*" K "]\n\t" \
    "v_lshlrev_b32 v36, 16, v[42+4*" K "]\n\t" \
    "v_and_b32 v37, %[mask], v[42+4*" K "]\n\t" \
    "v_lshlrev_b32 v38, 16, v[43+4*" K "]\n\t" \
    "v_and_b32 v39, %[mask], v[43+4*" K "]\n\t"
#define ACCUM(K) UNPACK(K) \
    "v_pk_add_f32 v[24:25], v[24:25], v[32:33]\n\t" \
    "v_pk_add_f32 v[26:27], v[26:27], v[34:35]\n\t" \
    "v_pk_add_f32 v[28:29], v[28:29], v[36:37]\n\t" \
    "v_pk_add_f32 v[30:31], v[30:31], v[38:39]\n\t"

__global__ void __launch_bounds__(NT, 3) __attribute__((amdgpu_num_vgpr(20)))
se_kernel(const void* x_, const void* wd_, const void* wu_, void* out_, void* ws_) {
    __shared__ __attribute__((aligned(16))) char lds[L_END];
    u64x2 k01, k23; u64 k4;
    asm volatile("s_load_dwordx4 %0, %3, 0x0\n\ts_load_dwordx4 %1, %3, 0x10\n\ts_load_dwordx2 %2, %3, 0x20\n\ts_waitcnt lgkmcnt(0)"
                 : "=&s"(k01), "=&s"(k23), "=&s"(k4) : "s"(__builtin_amdgcn_kernarg_segment_ptr()));
    const u64 x = k01[0], wd = k01[1], wu = k23[0], out = k23[1], ws = k4;

    const int t = threadIdx.x;
    const int lane = t & 63;
    const int w = __builtin_amdgcn_readfirstlane(t >> 6);
    const int b = blockIdx.x;
    const u32 even_xcc = 1u - (__builtin_amdgcn_s_getreg((3 << 11) | 20) & 1u);
    STAMP_AT(0);

    const u32 reg = (b & 7) * 32 + (b >> 3);
    const u64 base = (u64)reg * (NT * 16);
    const u32 span = 31u * STEP + NT * 16;
    const auto rx = __builtin_amdgcn_make_buffer_rsrc((void*)(x + base), 0, span, 0x00020000);
    const u32 voff = t * 16;

    int t2 = t;
    asm volatile(
        ".set se_i, 0\n\t"
        ".rept " STR(FIRST) "\n\t"
        "s_mov_b32 s63, se_i*" STR(STEP) "\n\t"
        "buffer_load_dwordx4 v[40+4*se_i:43+4*se_i], %[voff], %[rx], s63 offen" LD_POL "\n\t"
        ".set se_i, se_i+1\n\t"
        ".endr"
        : "+v"(t2) : [voff] "v"(voff), [rx] "s"(rx) : DATA_REGS, "s63", "memory");

    const u32 lb = (u32)(uintptr_t)(LAS char*)lds;
    const int lane2 = t2 & 63;
    const int q = lane2 >> 4;
    const u32 slot = 16 * w + (lane2 & 15);
    const u32 pl = slot >= 96 ? 1 : 0, idx = slot - 96 * pl;
    const u32 ch = 8 * (idx >> 2) + 4 * pl + (idx & 3);
    const u64 wp = t2 < 288 ? wu + t2 * 16 : ws;
    const u64 wdp = wd + (ch * R + 3 * q) * 2;
    u32 a_wl = lb + L_W + t2 * 16;
    u32 a_red = lb + L_RED + (t2 / 24) * RS + (t2 % 24) * 16;
    u32 a_rd = lb + L_RED + pl * PL + q * RS + idx * 4;
    u32 a_part = lb + L_PART + (3 * q) * 64 + w * 4;
    u32 a_sv = lb + L_SV + (t2 % 24) * 32;
    if (t2 < 64) *lds_at<float>(lb + L_PART + (t2 >> 2) * 64 + 48 + (t2 & 3) * 4) = 0.f;

    f32x4 accA, accB;
    u32 w0, w1, w2, dummy;
    const auto ro = __builtin_amdgcn_make_buffer_rsrc((void*)(out + base), 0, span, 0x00020000);
    const u32 woff = (t2 & 31) * STEP;
    {
        u32x4 wv;
        u32 so;
        asm volatile(
            "s_cmp_lg_u32 %[thr], 0\n\t"
            "global_load_dwordx4 %[wv], %[wp], off\n\t"
            "global_load_ushort %[w0], %[wdp], off\n\t"
            "global_load_ushort %[w1], %[wdp], off offset:2\n\t"
            "global_load_ushort %[w2], %[wdp], off offset:4\n\t"
            "v_mov_b32 v24, 0\n\tv_mov_b32 v25, 0\n\tv_mov_b32 v26, 0\n\tv_mov_b32 v27, 0\n\t"
            "v_mov_b32 v28, 0\n\tv_mov_b32 v29, 0\n\tv_mov_b32 v30, 0\n\tv_mov_b32 v31, 0\n\t"
            ".set se_i, " STR(FIRST) "\n\t"
            ".rept 32-" STR(FIRST) "\n\t"
            ".if se_i >= " STR(WIN_E) "\n\t"
            "s_cbranch_scc0 6f\n\t"
            "s_waitcnt vmcnt(" STR(WIN_E) "-1)\n"
            "6:\n\t"
            ".endif\n\t"
            "s_mov_b32 %[so], se_i*" STR(STEP) "\n\t"
            "buffer_load_dwordx4 v[40+4*se_i:43+4*se_i], %[voff], %[rx], %[so] offen" LD_POL "\n\t"
            ".if se_i == " STR(WARM_AT) "\n\t"
            "s_cmp_lg_u32 %[w], " STR(WARM_WAVE) "\n\t"
            "s_cbranch_scc1 8f\n\t"
            "buffer_load_dword %[dummy], %[woff], %[ro], 0 offen" WARM_POL "\n"
            "8:\n\t"
            "s_cmp_lg_u32 %[thr], 0\n\t"
            ".endif\n\t"
            ".if se_i >= " STR(LAG) "\n\t"
            ".set se_k, se_i-" STR(LAG) "\n\t"
            "s_waitcnt vmcnt(" STR(LAG) ")\n\t"
            ".if se_k == 4\n\t"
            "ds_write_b128 %[awl], %[wv]\n\t"
            ".endif\n\t"
            ACCUM("se_k")
            ".endif\n\t"
            ".set se_i, se_i+1\n\t"
            ".endr\n\t"
            ".set se_k, 32-" STR(LAG) "\n\t"
            ".rept " STR(LAG) "\n\t"
            "s_waitcnt vmcnt(31-se_k)\n\t"
            ACCUM("se_k")
            ".set se_k, se_k+1\n\t"
            ".endr\n\t"
            "ds_write_b128 %[ared], v[24:27]\n\t"
            "ds_write_b128 %[ared], v[28:31] offset:" STR(PL) "\n\t"
            "s_waitcnt lgkmcnt(0)\n\t"
            "s_barrier"
            : "=&{v[24:27]}"(accA), "=&{v[28:31]}"(accB), [wv] "=&v"(wv), [w0] "=&v"(w0), [w1] "=&v"(w1), [w2] "=&v"(w2), [so] "=&s"(so), [dummy] "=&v"(dummy),
              [ard] "+v"(a_rd), [apart] "+v"(a_part), [asv] "+v"(a_sv)
            : [voff] "v"(voff), [rx] "s"(rx), [wp] "v"(wp), [wdp] "v"(wdp), [awl] "v"(a_wl), [ared] "v"(a_red),
              [mask] "s"(0xffff0000u), [thr] "s"(even_xcc), [w] "s"(w), [woff] "v"(woff), [ro] "s"(ro)
            : DATA_REGS, TMP_REGS, "scc", "memory");
    }
    STAMP_AT(1);
    u32 tag = *lds_at<u32>(lb + L_W + 288 * 16) + 1;
    {
        const LAS float* src = lds_at<float>(a_rd);
        constexpr int S = 4 * RS / 4;
        float v0 = src[0], v1 = src[S], v2 = src[2 * S], v3 = src[3 * S], v4 = src[4 * S], v5 = src[5 * S], v6 = src[6 * S], v7 = src[7 * S];
        float sc = ((v0 + v1) + (v2 + v3)) + ((v4 + v5) + (v6 + v7));
        sc = add_xor32(add_xor16(sc));
        float p0 = sc * bf_lo(w0), p1 = sc * bf_lo(w1), p2 = sc * bf_lo(w2);
        p0 = dpp_add<0x111>(p0); p1 = dpp_add<0x111>(p1); p2 = dpp_add<0x111>(p2);
        p0 = dpp_add<0x112>(p0); p1 = dpp_add<0x112>(p1); p2 = dpp_add<0x112>(p2);
        p0 = dpp_add<0x114>(p0); p1 = dpp_add<0x114>(p1); p2 = dpp_add<0x114>(p2);
        p0 = dpp_add<0x118>(p0); p1 = dpp_add<0x118>(p1); p2 = dpp_add<0x118>(p2);
        if ((lane & 15) == 15) {
            LAS float* dst = lds_at<float>(a_part);
            dst[0] = p0; dst[16] = p1; dst[32] = p2;
        }
    }
    const u64 gline = ws + 4096 + (u64)b * 128;
    u32 a_pp = lb + L_PART + (lane & 7) * 128 + (lane >> 3) * 4;
    u32 goff = (lane >> 3) * (NWG * 128) + (lane & 7) * 16;
    asm volatile("" : "+v"(tag), "+v"(a_pp), "+v"(goff));
    {
        asm volatile(
            "s_waitcnt lgkmcnt(0)\n\t"
            "s_barrier\n\t"
            "s_cmp_lg_u32 %[w], 0\n\t"
            "s_cbranch_scc1 1f\n\t"
            "ds_read2_b32 v[24:25], %[app] offset0:0 offset1:8\n\t"
            "ds_read2_b32 v[26:27], %[app] offset0:16 offset1:24\n\t"
            "v_mov_b32 v29, %[tag]\n\t"
            "v_mov_b32 v31, %[tag]\n\t"
            "s_waitcnt lgkmcnt(1)\n\t"
            "v_add_f32 v28, v24, v25\n\t"
            "s_waitcnt lgkmcnt(0)\n\t"
            "v_add_f32 v30, v26, v27\n\t"
            "s_nop 0\n\t"
            "v_add_f32_dpp v28, v28, v28 row_ror:8 row_mask:0xf bank_mask:0xf\n\t"
            "v_add_f32_dpp v30, v30, v30 row_ror:8 row_mask:0xf bank_mask:0xf\n\t"
            "s_nop 0\n\t"
            "v_mov_b32 v24, v28\n\t"
            "v_mov_b32 v25, v30\n\t"
            "s_nop 1\n\t"
            "v_permlane16_swap_b32 v28, v24\n\t"
            "v_permlane16_swap_b32 v30, v25\n\t"
            "s_nop 1\n\t"
            "v_add_f32 v28, v28, v24\n\t"
            "v_add_f32 v30, v30, v25\n\t"
            "v_mov_b32 v24, v28\n\t"
            "v_mov_b32 v25, v30\n\t"
            "s_nop 1\n\t"
            "v_permlane32_swap_b32 v28, v24\n\t"
            "v_permlane32_swap_b32 v30, v25\n\t"
            "s_nop 1\n\t"
            "v_add_f32 v28, v28, v24\n\t"
            "v_add_f32 v30, v30, v25\n\t"
            "global_store_dwordx4 %[goff], v[28:31], %[gline] sc1\n\t"
            "s_nop 1\n"
            "1:"
            :
            : [w] "s"(w), [app] "v"(a_pp), [tag] "v"(tag), [goff] "v"(goff), [gline] "s"(gline)
            : "v24", "v25", "v26", "v27", "v28", "v29", "v30", "v31", "scc", "memory");
    }
    STAMP_AT(2);
    float wuv[R];
    if (w < 3) {
        const LAS unsigned short* wul = lds_at<unsigned short>(lb + L_W + t * 2);
#pragma unroll
        for (int j = 0; j < R; ++j) wuv[j] = bf_lo(wul[j * C]);
    }
    if (w < 8) {
        const auto rg = __builtin_amdgcn_make_buffer_rsrc((void*)(ws + 4096 + (b & 7) * (NWG * 128)), 0, NWG * 128, 0x00020000);
        const u32 off = (32 * w + (lane >> 3)) * 128 + (lane & 7) * 16;
        u32x4 g0, g1, g2, g3;
        u64 m0 = 0x3f3f3f3f3f3f3f3full, m1 = m0, m2 = m0, m3 = m0, ma;
        u32 spin = SPIN_CAP;
        asm volatile(
            "s_sleep 4\n"
            "0:\n\t"
            "s_mov_b64 exec, %[m0]\n\t"
            "s_cbranch_execz 2f\n\t"
            "buffer_load_dwordx4 v[24:27], %[off], %[rg], 0 offen sc1\n"
            "2:\n\t"
            "s_mov_b64 exec, %[m1]\n\t"
            "s_cbranch_execz 3f\n\t"
            "buffer_load_dwordx4 v[28:31], %[off], %[rg], 0 offen offset:1024 sc1\n"
            "3:\n\t"
            "s_mov_b64 exec, %[m2]\n\t"
            "s_cbranch_execz 4f\n\t"
            "buffer_load_dwordx4 v[32:35], %[off], %[rg], 0 offen offset:2048 sc1\n"
            "4:\n\t"
            "s_mov_b64 exec, %[m3]\n\t"
            "s_cbranch_execz 5f\n\t"
            "buffer_load_dwordx4 v[36:39], %[off], %[rg], 0 offen offset:3072 sc1\n"
            "5:\n\t"
            "s_mov_b64 exec, -1\n\t"
            "s_waitcnt vmcnt(0)\n\t"
            "v_cmp_ne_u32 vcc, %[tag], v25\n\t"
            "v_cmp_ne_u32 %[ma], %[tag], v27\n\t"
            "s_or_b64 vcc, vcc, %[ma]\n\t"
            "s_and_b64 %[m0], %[m0], vcc\n\t"
            "v_cmp_ne_u32 vcc, %[tag], v29\n\t"
            "v_cmp_ne_u32 %[ma], %[tag], v31\n\t"
            "s_or_b64 vcc, vcc, %[ma]\n\t"
            "s_and_b64 %[m1], %[m1], vcc\n\t"
            "v_cmp_ne_u32 vcc, %[tag], v33\n\t"
            "v_cmp_ne_u32 %[ma], %[tag], v35\n\t"
            "s_or_b64 vcc, vcc, %[ma]\n\t"
            "s_and_b64 %[m2], %[m2], vcc\n\t"
            "v_cmp_ne_u32 vcc, %[tag], v37\n\t"
            "v_cmp_ne_u32 %[ma], %[tag], v39\n\t"
            "s_or_b64 vcc, vcc, %[ma]\n\t"
            "s_and_b64 %[m3], %[m3], vcc\n\t"
            "s_or_b64 %[ma], %[m0], %[m1]\n\t"
            "s_or_b64 vcc, %[m2], %[m3]\n\t"
            "s_or_b64 %[ma], %[ma], vcc\n\t"
            "s_cbranch_scc0 7f\n\t"
            "s_sub_u32 %[spin], %[spin], 1\n\t"
            "s_cbranch_scc0 0b\n"
            "7:"
            : "=&{v[24:27]}"(g0), "=&{v[28:31]}"(g1), "=&{v[32:35]}"(g2), "=&{v[36:39]}"(g3),
              [m0] "+s"(m0), [m1] "+s"(m1), [m2] "+s"(m2), [m3] "+s"(m3), [ma] "=&s"(ma), [spin] "+s"(spin)
            : [off] "v"(off), [rg] "s"(rg), [tag] "s"(__builtin_amdgcn_readfirstlane(tag))
            : "vcc", "scc", "memory");
        float z0 = (__uint_as_float(g0[0]) + __uint_as_float(g1[0])) + (__uint_as_float(g2[0]) + __uint_as_float(g3[0]));
        float z1 = (__uint_as_float(g0[2]) + __uint_as_float(g1[2])) + (__uint_as_float(g2[2]) + __uint_as_float(g3[2]));
        z0 = dpp_add<0x128>(z0); z1 = dpp_add<0x128>(z1);
        z0 = add_xor32(add_xor16(z0)); z1 = add_xor32(add_xor16(z1));
        if (lane < 6) { LAS float* d = lds_at<float>(lb + L_PART2 + lane * 64 + w * 4); d[0] = z0; d[8] = z1; }
    }
    __syncthreads();
    STAMP_AT(3);
    if (b == 0 && t == 0) __hip_atomic_store((gu32*)ws, tag, __ATOMIC_RELAXED, __HIP_MEMORY_SCOPE_AGENT);
    if (w < 3) {
        f32x4 pv = *lds_at<f32x4>(lb + L_PART2 + (lane & 31) * 16);
        float z = (pv[0] + pv[1]) + (pv[2] + pv[3]);
        z = dpp_add<0xb1>(z);
        z = bfround(z * (1.0f / (512.f * 512.f)));
        const int hb = __float_as_int(bfround(z * __builtin_amdgcn_rcpf(1.f + __expf(-z))));
        float ua = 0.f, ub = 0.f, uc = 0.f;
#pragma unroll
        for (int j = 0; j < 4; ++j) {
            ua += __int_as_float(__builtin_amdgcn_readlane(hb, 2 * j)) * wuv[j];
            ub += __int_as_float(__builtin_amdgcn_readlane(hb, 2 * (j + 4))) * wuv[j + 4];
            uc += __int_as_float(__builtin_amdgcn_readlane(hb, 2 * (j + 8))) * wuv[j + 8];
        }
        const float u = bfround((ua + ub) + uc);
        *lds_at<float>(lb + L_SV + t * 4) = bfround(__builtin_amdgcn_rcpf(1.f + __expf(-u)));
    }
    __syncthreads();
    {
        const f32x4 sa = *lds_at<f32x4>(a_sv), sb = *lds_at<f32x4>(a_sv + 16);
        u32 so;
        STAMP_AT(4);
        asm volatile(
            "s_memrealtime s[60:61]\n\t"
            "s_waitcnt lgkmcnt(0)\n\t"
            "s_add_u32 s62, s60, %[phase]\n\t"
            ".set se_i, 0\n\t"
            ".rept 32\n\t"
            UNPACK("se_i")
            "v_pk_mul_f32 v[32:33], v[32:33], v[24:25]\n\t"
            "v_pk_mul_f32 v[34:35], v[34:35], v[26:27]\n\t"
            "v_pk_mul_f32 v[36:37], v[36:37], v[28:29]\n\t"
            "v_pk_mul_f32 v[38:39], v[38:39], v[30:31]\n\t"
            "s_mov_b32 %[so], se_i*" STR(STEP) "\n\t"
            "v_cvt_pk_bf16_f32 v[40+4*se_i], v32, v33\n\t"
            "v_cvt_pk_bf16_f32 v[41+4*se_i], v34, v35\n\t"
            "v_cvt_pk_bf16_f32 v[42+4*se_i], v36, v37\n\t"
            "v_cvt_pk_bf16_f32 v[43+4*se_i], v38, v39\n\t"
            "s_add_u32 s63, s62, (se_i*" STR(PACE) ")/32\n"
            "9:\n\t"
            "s_memrealtime s[60:61]\n\t"
            "s_waitcnt lgkmcnt(0)\n\t"
            "s_sub_u32 s61, s60, s63\n\t"
            "s_cmp_lt_i32 s61, 0\n\t"
            "s_cbranch_scc1 9b\n\t"
            "buffer_store_dwordx4 v[40+4*se_i:43+4*se_i], %[voff], %[ro], %[so] offen" ST_POL "\n\t"
            ".set se_i, se_i+1\n\t"
            ".endr\n\t"
            : [so] "=&s"(so)
            : "{v[24:27]}"(sa), "{v[28:31]}"(sb), [voff] "v"(voff), [ro] "s"(ro), [mask] "s"(0xffff0000u),
              [phase] "s"(even_xcc ? (u32)(w * (PACE / 32) / 12) : (u32)-100000)
            : DATA_REGS, TMP_REGS, "s60", "s61", "s62", "s63", "scc", "memory");
    }
}

extern "C" void kernel_launch(void* const* d_in, const int* in_sizes, int n_in,
                              void* d_out, int out_size, void* d_ws, size_t ws_size,
                              hipStream_t stream) {
    se_kernel<<<NWG, NT, 0, stream>>>(d_in[0], d_in[1], d_in[2], d_out, d_ws);
}
```

```cpp
#include <hip/hip_runtime.h>
#include <hip/hip_bf16.h>
#include <stdint.h>

typedef unsigned int u32;
typedef unsigned long long u64;
typedef u32 u32x4 __attribute__((ext_vector_type(4)));
typedef float f32x2 __attribute__((ext_vector_type(2)));
typedef float f32x4 __attribute__((ext_vector_type(4)));
typedef u64 u64x2 __attribute__((ext_vector_type(2)));
typedef __attribute__((address_space(1))) u32 gu32;
typedef __attribute__((address_space(1))) u64 gu64;
#define LAS __attribute__((address_space(3)))

#define C 192
#define R 12
#define NWG 256
#define NT 768
#define STEP 3145728
#define LD_POL " nt"
#define ST_POL " sc0 sc1"
#define SPIN_CAP (1u << 20)
#define LAG 8
#define FIRST 4
#define WARM_AT 16
#define WARM_WAVE 11
#define WARM_POL ""
#define XWARM 1
#define XWARM_WAVE 10
#define XW_CLAMP 0xffffffffu
#define WIN_E 32
#define PACE 1240
#define STR2(x) #x
#define STR(x) STR2(x)

#define CL10(p) "v" #p "0","v" #p "1","v" #p "2","v" #p "3","v" #p "4","v" #p "5","v" #p "6","v" #p "7","v" #p "8","v" #p "9"
#define DATA_REGS CL10(4), CL10(5), CL10(6), CL10(7), CL10(8), CL10(9), CL10(10), CL10(11), CL10(12), CL10(13), CL10(14), CL10(15), \
    "v160","v161","v162","v163","v164","v165","v166","v167"
#define TMP_REGS "v32","v33","v34","v35","v36","v37","v38","v39"

__device__ __forceinline__ float bf_lo(u32 v) { return __uint_as_float(v << 16); }
__device__ __forceinline__ float bfround(float f) {
    typedef __bf16 bf16x2 __attribute__((ext_vector_type(2)));
    f32x2 v = {f, f};
    return bf_lo(__builtin_bit_cast(u32, __builtin_convertvector(v, bf16x2)));
}
template <int CTRL>
__device__ __forceinline__ float dpp_add(float v) {
    return v + __int_as_float(__builtin_amdgcn_update_dpp(0, __float_as_int(v), CTRL, 0xf, 0xf, true));
}
__device__ __forceinline__ float add_xor16(float v) {
    auto r = __builtin_amdgcn_permlane16_swap(__float_as_uint(v), __float_as_uint(v), false, false);
    return __uint_as_float(r[0]) + __uint_as_float(r[1]);
}
__device__ __forceinline__ float add_xor32(float v) {
    auto r = __builtin_amdgcn_permlane32_swap(__float_as_uint(v), __float_as_uint(v), false, false);
    return __uint_as_float(r[0]) + __uint_as_float(r[1]);
}
template <typename T>
__device__ __forceinline__ LAS T* lds_at(u32 byte) { return (LAS T*)(uintptr_t)byte; }

#define STAMP_AT(k) do { } while (0)

#define RS 448
#define PL (32 * RS)
#define L_RED 0
#define L_PART (2 * PL)
#define L_PART2 (L_PART + 1024)
#define L_SV (L_PART2 + 512)
#define L_W (L_SV + 768)
#define L_END (L_W + NT * 16)

#define UNPACK(K) \
    "v_lshlrev_b32 v32, 16, v[40+4*" K "]\n\t" \
    "v_and_b32 v33, %[mask], v[40+4*" K "]\n\t" \
    "v_lshlrev_b32 v34, 16, v[41+4*" K "]\n\t" \
    "v_and_b32 v35, %[mask], v[41+4*" K "]\n\t" \
    "v_lshlrev_b32 v36, 16, v[42+4*" K "]\n\t" \
    "v_and_b32 v37, %[mask], v[42+4*" K "]\n\t" \
    "v_lshlrev_b32 v38, 16, v[43+4*" K "]\n\t" \
    "v_and_b32 v39, %[mask], v[43+4*" K "]\n\t"
#define ACCUM(K) UNPACK(K) \
    "v_pk_add_f32 v[24:25], v[24:25], v[32:33]\n\t" \
    "v_pk_add_f32 v[26:27], v[26:27], v[34:35]\n\t" \
    "v_pk_add_f32 v[28:29], v[28:29], v[36:37]\n\t" \
    "v_pk_add_f32 v[30:31], v[30:31], v[38:39]\n\t"

__global__ void __launch_bounds__(NT, 3) __attribute__((amdgpu_num_vgpr(20)))
se_kernel(const void* x_, const void* wd_, const void* wu_, void* out_, void* ws_) {
    __shared__ __attribute__((aligned(16))) char lds[L_END];
    u64x2 k01, k23; u64 k4;
    asm volatile("s_load_dwordx4 %0, %3, 0x0\n\ts_load_dwordx4 %1, %3, 0x10\n\ts_load_dwordx2 %2, %3, 0x20\n\ts_waitcnt lgkmcnt(0)"
                 : "=&s"(k01), "=&s"(k23), "=&s"(k4) : "s"(__builtin_amdgcn_kernarg_segment_ptr()));
    const u64 x = k01[0], wd = k01[1], wu = k23[0], out = k23[1], ws = k4;

    const int t = threadIdx.x;
    const int lane = t & 63;
    const int w = __builtin_amdgcn_readfirstlane(t >> 6);
    const int b = blockIdx.x;
    const u32 even_xcc = 1u - (__builtin_amdgcn_s_getreg((3 << 11) | 20) & 1u);
    STAMP_AT(0);

    const u32 reg = (b & 7) * 32 + (b >> 3);
    const u64 base = (u64)reg * (NT * 16);
    const u32 span = 31u * STEP + NT * 16;
    const auto rx = __builtin_amdgcn_make_buffer_rsrc((void*)(x + base), 0, span, 0x00020000);
    const u32 voff = t * 16;

    int t2 = t;
    asm volatile(
        ".set se_i, 0\n\t"
        ".rept " STR(FIRST) "\n\t"
        "s_mov_b32 s63, se_i*" STR(STEP) "\n\t"
        "buffer_load_dwordx4 v[40+4*se_i:43+4*se_i], %[voff], %[rx], s63 offen" LD_POL "\n\t"
        ".set se_i, se_i+1\n\t"
        ".endr"
        : "+v"(t2) : [voff] "v"(voff), [rx] "s"(rx) : DATA_REGS, "s63", "memory");

    const u32 lb = (u32)(uintptr_t)(LAS char*)lds;
    const int lane2 = t2 & 63;
    const int q = lane2 >> 4;
    const u32 slot = 16 * w + (lane2 & 15);
    const u32 pl = slot >= 96 ? 1 : 0, idx = slot - 96 * pl;
    const u32 ch = 8 * (idx >> 2) + 4 * pl + (idx & 3);
    const u64 wp = t2 < 288 ? wu + t2 * 16 : ws;
    const u64 wdp = wd + (ch * R + 3 * q) * 2;
    u32 a_wl = lb + L_W + t2 * 16;
    u32 a_red = lb + L_RED + (t2 / 24) * RS + (t2 % 24) * 16;
    u32 a_rd = lb + L_RED + pl * PL + q * RS + idx * 4;
    u32 a_part = lb + L_PART + (3 * q) * 64 + w * 4;
    u32 a_sv = lb + L_SV + (t2 % 24) * 32;
    if (t2 < 64) *lds_at<float>(lb + L_PART + (t2 >> 2) * 64 + 48 + (t2 & 3) * 4) = 0.f;

    f32x4 accA, accB;
    u32 w0, w1, w2, dummy, dummy2;
    const auto ro = __builtin_amdgcn_make_buffer_rsrc((void*)(out + base), 0, span, 0x00020000);
    const u32 woff = (t2 & 31) * STEP;
    const u32 xwoff = woff + NT * 16 - 128 > XW_CLAMP ? woff : woff;
    {
        u32x4 wv;
        u32 so;
        asm volatile(
            "s_cmp_lg_u32 %[thr], 0\n\t"
            "global_load_dwordx4 %[wv], %[wp], off\n\t"
            "global_load_ushort %[w0], %[wdp], off\n\t"
            "global_load_ushort %[w1], %[wdp], off offset:2\n\t"
            "global_load_ushort %[w2], %[wdp], off offset:4\n\t"
            "s_cmp_lg_u32 %[w], " STR(XWARM_WAVE) "\n\t"
            "s_cbranch_scc1 8f\n\t"
            "buffer_load_dword %[dummy2], %[xwoff], %[rx], 0 offen" LD_POL "\n"
            "8:\n\t"
            "s_cmp_lg_u32 %[thr], 0\n\t"
            "v_mov_b32 v24, 0\n\tv_mov_b32 v25, 0\n\tv_mov_b32 v26, 0\n\tv_mov_b32 v27, 0\n\t"
            "v_mov_b32 v28, 0\n\tv_mov_b32 v29, 0\n\tv_mov_b32 v30, 0\n\tv_mov_b32 v31, 0\n\t"
            ".set se_i, " STR(FIRST) "\n\t"
            ".rept 32-" STR(FIRST) "\n\t"
            ".if se_i >= " STR(WIN_E) "\n\t"
            "s_cbranch_scc0 6f\n\t"
            "s_waitcnt vmcnt(" STR(WIN_E) "-1)\n"
            "6:\n\t"
            ".endif\n\t"
            "s_mov_b32 %[so], se_i*" STR(STEP) "\n\t"
            "buffer_load_dwordx4 v[40+4*se_i:43+4*se_i], %[voff], %[rx], %[so] offen" LD_POL "\n\t"
            ".if se_i == " STR(WARM_AT) "\n\t"
            "s_cmp_lg_u32 %[w], " STR(WARM_WAVE) "\n\t"
            "s_cbranch_scc1 8f\n\t"
            "buffer_load_dword %[dummy], %[woff], %[ro], 0 offen" WARM_POL "\n"
            "8:\n\t"
            "s_cmp_lg_u32 %[thr], 0\n\t"
            ".endif\n\t"
            ".if se_i >= " STR(LAG) "\n\t"
            ".set se_k, se_i-" STR(LAG) "\n\t"
            "s_waitcnt vmcnt(" STR(LAG) ")\n\t"
            ".if se_k == 4\n\t"
            "ds_write_b128 %[awl], %[wv]\n\t"
            ".endif\n\t"
            ACCUM("se_k")
            ".endif\n\t"
            ".set se_i, se_i+1\n\t"
            ".endr\n\t"
            ".set se_k, 32-" STR(LAG) "\n\t"
            ".rept " STR(LAG) "\n\t"
            "s_waitcnt vmcnt(31-se_k)\n\t"
            ACCUM("se_k")
            ".set se_k, se_k+1\n\t"
            ".endr\n\t"
            "ds_write_b128 %[ared], v[24:27]\n\t"
            "ds_write_b128 %[ared], v[28:31] offset:" STR(PL) "\n\t"
            "s_waitcnt lgkmcnt(0)\n\t"
            "s_barrier"
            : "=&{v[24:27]}"(accA), "=&{v[28:31]}"(accB), [wv] "=&v"(wv), [w0] "=&v"(w0), [w1] "=&v"(w1), [w2] "=&v"(w2), [so] "=&s"(so), [dummy] "=&v"(dummy), [dummy2] "=&v"(dummy2),
              [ard] "+v"(a_rd), [apart] "+v"(a_part), [asv] "+v"(a_sv)
            : [voff] "v"(voff), [rx] "s"(rx), [wp] "v"(wp), [wdp] "v"(wdp), [awl] "v"(a_wl), [ared] "v"(a_red),
              [mask] "s"(0xffff0000u), [thr] "s"(even_xcc), [w] "s"(w), [woff] "v"(woff), [xwoff] "v"(xwoff), [ro] "s"(ro)
            : DATA_REGS, TMP_REGS, "scc", "memory");
    }
    STAMP_AT(1);
    u32 tag = *lds_at<u32>(lb + L_W + 288 * 16) + 1;
    {
        const LAS float* src = lds_at<float>(a_rd);
        constexpr int S = 4 * RS / 4;
        float v0 = src[0], v1 = src[S], v2 = src[2 * S], v3 = src[3 * S], v4 = src[4 * S], v5 = src[5 * S], v6 = src[6 * S], v7 = src[7 * S];
        float sc = ((v0 + v1) + (v2 + v3)) + ((v4 + v5) + (v6 + v7));
        sc = add_xor32(add_xor16(sc));
        float p0 = sc * bf_lo(w0), p1 = sc * bf_lo(w1), p2 = sc * bf_lo(w2);
        p0 = dpp_add<0x111>(p0); p1 = dpp_add<0x111>(p1); p2 = dpp_add<0x111>(p2);
        p0 = dpp_add<0x112>(p0); p1 = dpp_add<0x112>(p1); p2 = dpp_add<0x112>(p2);
        p0 = dpp_add<0x114>(p0); p1 = dpp_add<0x114>(p1); p2 = dpp_add<0x114>(p2);
        p0 = dpp_add<0x118>(p0); p1 = dpp_add<0x118>(p1); p2 = dpp_add<0x118>(p2);
        if ((lane & 15) == 15) {
            LAS float* dst = lds_at<float>(a_part);
            dst[0] = p0; dst[16] = p1; dst[32] = p2;
        }
    }
    const u64 gline = ws + 4096 + (u64)b * 128;
    u32 a_pp = lb + L_PART + (lane & 7) * 128 + (lane >> 3) * 4;
    u32 goff = (lane >> 3) * (NWG * 128) + (lane & 7) * 16;
    asm volatile("" : "+v"(tag), "+v"(a_pp), "+v"(goff));
    {
        asm volatile(
            "s_waitcnt lgkmcnt(0)\n\t"
            "s_barrier\n\t"
            "s_cmp_lg_u32 %[w], 0\n\t"
            "s_cbranch_scc1 1f\n\t"
            "ds_read2_b32 v[24:25], %[app] offset0:0 offset1:8\n\t"
            "ds_read2_b32 v[26:27], %[app] offset0:16 offset1:24\n\t"
            "v_mov_b32 v29, %[tag]\n\t"
            "v_mov_b32 v31, %[tag]\n\t"
            "s_waitcnt lgkmcnt(1)\n\t"
            "v_add_f32 v28, v24, v25\n\t"
            "s_waitcnt lgkmcnt(0)\n\t"
            "v_add_f32 v30, v26, v27\n\t"
            "s_nop 0\n\t"
            "v_add_f32_dpp v28, v28, v28 row_ror:8 row_mask:0xf bank_mask:0xf\n\t"
            "v_add_f32_dpp v30, v30, v30 row_ror:8 row_mask:0xf bank_mask:0xf\n\t"
            "s_nop 0\n\t"
            "v_mov_b32 v24, v28\n\t"
            "v_mov_b32 v25, v30\n\t"
            "s_nop 1\n\t"
            "v_permlane16_swap_b32 v28, v24\n\t"
            "v_permlane16_swap_b32 v30, v25\n\t"
            "s_nop 1\n\t"
            "v_add_f32 v28, v28, v24\n\t"
            "v_add_f32 v30, v30, v25\n\t"
            "v_mov_b32 v24, v28\n\t"
            "v_mov_b32 v25, v30\n\t"
            "s_nop 1\n\t"
            "v_permlane32_swap_b32 v28, v24\n\t"
            "v_permlane32_swap_b32 v30, v25\n\t"
            "s_nop 1\n\t"
            "v_add_f32 v28, v28, v24\n\t"
            "v_add_f32 v30, v30, v25\n\t"
            "global_store_dwordx4 %[goff], v[28:31], %[gline] sc1\n\t"
            "s_nop 1\n"
            "1:"
            :
            : [w] "s"(w), [app] "v"(a_pp), [tag] "v"(tag), [goff] "v"(goff), [gline] "s"(gline)
            : "v24", "v25", "v26", "v27", "v28", "v29", "v30", "v31", "scc", "memory");
    }
    STAMP_AT(2);
    float wuv[R];
    if (w < 3) {
        const LAS unsigned short* wul = lds_at<unsigned short>(lb + L_W + t * 2);
#pragma unroll
        for (int j = 0; j < R; ++j) wuv[j] = bf_lo(wul[j * C]);
    }
    if (w < 8) {
        const auto rg = __builtin_amdgcn_make_buffer_rsrc((void*)(ws + 4096 + (b & 7) * (NWG * 128)), 0, NWG * 128, 0x00020000);
        const u32 off = (32 * w + (lane >> 3)) * 128 + (lane & 7) * 16;
        u32x4 g0, g1, g2, g3;
        u64 m0 = 0x3f3f3f3f3f3f3f3full, m1 = m0, m2 = m0, m3 = m0, ma;
        u32 spin = SPIN_CAP;
        asm volatile(
            "s_sleep 4\n"
            "0:\n\t"
            "s_mov_b64 exec, %[m0]\n\t"
            "s_cbranch_execz 2f\n\t"
            "buffer_load_dwordx4 v[24:27], %[off], %[rg], 0 offen sc1\n"
            "2:\n\t"
            "s_mov_b64 exec, %[m1]\n\t"
            "s_cbranch_execz 3f\n\t"
            "buffer_load_dwordx4 v[28:31], %[off], %[rg], 0 offen offset:1024 sc1\n"
            "3:\n\t"
            "s_mov_b64 exec, %[m2]\n\t"
            "s_cbranch_execz 4f\n\t"
            "buffer_load_dwordx4 v[32:35], %[off], %[rg], 0 offen offset:2048 sc1\n"
            "4:\n\t"
            "s_mov_b64 exec, %[m3]\n\t"
            "s_cbranch_execz 5f\n\t"
            "buffer_load_dwordx4 v[36:39], %[off], %[rg], 0 offen offset:3072 sc1\n"
            "5:\n\t"
            "s_mov_b64 exec, -1\n\t"
            "s_waitcnt vmcnt(0)\n\t"
            "v_cmp_ne_u32 vcc, %[tag], v25\n\t"
            "v_cmp_ne_u32 %[ma], %[tag], v27\n\t"
            "s_or_b64 vcc, vcc, %[ma]\n\t"
            "s_and_b64 %[m0], %[m0], vcc\n\t"
            "v_cmp_ne_u32 vcc, %[tag], v29\n\t"
            "v_cmp_ne_u32 %[ma], %[tag], v31\n\t"
            "s_or_b64 vcc, vcc, %[ma]\n\t"
            "s_and_b64 %[m1], %[m1], vcc\n\t"
            "v_cmp_ne_u32 vcc, %[tag], v33\n\t"
            "v_cmp_ne_u32 %[ma], %[tag], v35\n\t"
            "s_or_b64 vcc, vcc, %[ma]\n\t"
            "s_and_b64 %[m2], %[m2], vcc\n\t"
            "v_cmp_ne_u32 vcc, %[tag], v37\n\t"
            "v_cmp_ne_u32 %[ma], %[tag], v39\n\t"
            "s_or_b64 vcc, vcc, %[ma]\n\t"
            "s_and_b64 %[m3], %[m3], vcc\n\t"
            "s_or_b64 %[ma], %[m0], %[m1]\n\t"
            "s_or_b64 vcc, %[m2], %[m3]\n\t"
            "s_or_b64 %[ma], %[ma], vcc\n\t"
            "s_cbranch_scc0 7f\n\t"
            "s_sub_u32 %[spin], %[spin], 1\n\t"
            "s_cbranch_scc0 0b\n"
            "7:"
            : "=&{v[24:27]}"(g0), "=&{v[28:31]}"(g1), "=&{v[32:35]}"(g2), "=&{v[36:39]}"(g3),
              [m0] "+s"(m0), [m1] "+s"(m1), [m2] "+s"(m2), [m3] "+s"(m3), [ma] "=&s"(ma), [spin] "+s"(spin)
            : [off] "v"(off), [rg] "s"(rg), [tag] "s"(__builtin_amdgcn_readfirstlane(tag))
            : "vcc", "scc", "memory");
        float z0 = (__uint_as_float(g0[0]) + __uint_as_float(g1[0])) + (__uint_as_float(g2[0]) + __uint_as_float(g3[0]));
        float z1 = (__uint_as_float(g0[2]) + __uint_as_float(g1[2])) + (__uint_as_float(g2[2]) + __uint_as_float(g3[2]));
        z0 = dpp_add<0x128>(z0); z1 = dpp_add<0x128>(z1);
        z0 = add_xor32(add_xor16(z0)); z1 = add_xor32(add_xor16(z1));
        if (lane < 6) { LAS float* d = lds_at<float>(lb + L_PART2 + lane * 64 + w * 4); d[0] = z0; d[8] = z1; }
    }
    __syncthreads();
    STAMP_AT(3);
    if (b == 0 && t == 0) __hip_atomic_store((gu32*)ws, tag, __ATOMIC_RELAXED, __HIP_MEMORY_SCOPE_AGENT);
    if (w < 3) {
        f32x4 pv = *lds_at<f32x4>(lb + L_PART2 + (lane & 31) * 16);
        float z = (pv[0] + pv[1]) + (pv[2] + pv[3]);
        z = dpp_add<0xb1>(z);
        z = bfround(z * (1.0f / (512.f * 512.f)));
        const int hb = __float_as_int(bfround(z * __builtin_amdgcn_rcpf(1.f + __expf(-z))));
        float ua = 0.f, ub = 0.f, uc = 0.f;
#pragma unroll
        for (int j = 0; j < 4; ++j) {
            ua += __int_as_float(__builtin_amdgcn_readlane(hb, 2 * j)) * wuv[j];
            ub += __int_as_float(__builtin_amdgcn_readlane(hb, 2 * (j + 4))) * wuv[j + 4];
            uc += __int_as_float(__builtin_amdgcn_readlane(hb, 2 * (j + 8))) * wuv[j + 8];
        }
        const float u = bfround((ua + ub) + uc);
        *lds_at<float>(lb + L_SV + t * 4) = bfround(__builtin_amdgcn_rcpf(1.f + __expf(-u)));
    }
    __syncthreads();
    {
        const f32x4 sa = *lds_at<f32x4>(a_sv), sb = *lds_at<f32x4>(a_sv + 16);
        u32 so;
        STAMP_AT(4);
        asm volatile(
            "s_memrealtime s[60:61]\n\t"
            "s_waitcnt lgkmcnt(0)\n\t"
            "s_add_u32 s62, s60, %[phase]\n\t"
            ".set se_i, 0\n\t"
            ".rept 32\n\t"
            UNPACK("se_i")
            "v_pk_mul_f32 v[32:33], v[32:33], v[24:25]\n\t"
            "v_pk_mul_f32 v[34:35], v[34:35], v[26:27]\n\t"
            "v_pk_mul_f32 v[36:37], v[36:37], v[28:29]\n\t"
            "v_pk_mul_f32 v[38:39], v[38:39], v[30:31]\n\t"
            "s_mov_b32 %[so], se_i*" STR(STEP) "\n\t"
            "v_cvt_pk_bf16_f32 v[40+4*se_i], v32, v33\n\t"
            "v_cvt_pk_bf16_f32 v[41+4*se_i], v34, v35\n\t"
            "v_cvt_pk_bf16_f32 v[42+4*se_i], v36, v37\n\t"
            "v_cvt_pk_bf16_f32 v[43+4*se_i], v38, v39\n\t"
            "s_add_u32 s63, s62, (se_i*" STR(PACE) ")/32\n"
            "9:\n\t"
            "s_memrealtime s[60:61]\n\t"
            "s_waitcnt lgkmcnt(0)\n\t"
            "s_sub_u32 s61, s60, s63\n\t"
            "s_cmp_lt_i32 s61, 0\n\t"
            "s_cbranch_scc1 9b\n\t"
            "buffer_store_dwordx4 v[40+4*se_i:43+4*se_i], %[voff], %[ro], %[so] offen" ST_POL "\n\t"
            ".set se_i, se_i+1\n\t"
            ".endr\n\t"
            : [so] "=&s"(so)
            : "{v[24:27]}"(sa), "{v[28:31]}"(sb), [voff] "v"(voff), [ro] "s"(ro), [mask] "s"(0xffff0000u),
              [phase] "s"(even_xcc ? (u32)(w * (PACE / 32) / 12) : (u32)-100000)
            : DATA_REGS, TMP_REGS, "s60", "s61", "s62", "s63", "scc", "memory");
    }
}

extern "C" void kernel_launch(void* const* d_in, const int* in_sizes, int n_in,
                              void* d_out, int out_size, void* d_ws, size_t ws_size,
                              hipStream_t stream) {
    se_kernel<<<NWG, NT, 0, stream>>>(d_in[0], d_in[1], d_in[2], d_out, d_ws);
}
```

```cpp
#include <hip/hip_runtime.h>
#include <hip/hip_bf16.h>
#include <stdint.h>

typedef unsigned int u32;
typedef unsigned long long u64;
typedef u32 u32x4 __attribute__((ext_vector_type(4)));
typedef float f32x2 __attribute__((ext_vector_type(2)));
typedef float f32x4 __attribute__((ext_vector_type(4)));
typedef u64 u64x2 __attribute__((ext_vector_type(2)));
typedef __attribute__((address_space(1))) u32 gu32;
typedef __attribute__((address_space(1))) u64 gu64;
#define LAS __attribute__((address_space(3)))

#define C 192
#define R 12
#define NWG 256
#define NT 768
#define STEP 3145728
#define LD_POL " nt"
#define ST_POL " sc0 sc1"
#define SPIN_CAP (1u << 20)
#define LAG 6
#define FIRST 4
#define WARM_AT 20
#define WARM_WAVE 11
#define WARM_POL ""
#define WIN_E 32
#define PACE 1240
#define STR2(x) #x
#define STR(x) STR2(x)

#define CL10(p) "v" #p "0","v" #p "1","v" #p "2","v" #p "3","v" #p "4","v" #p "5","v" #p "6","v" #p "7","v" #p "8","v" #p "9"
#define DATA_REGS CL10(4), CL10(5), CL10(6), CL10(7), CL10(8), CL10(9), CL10(10), CL10(11), CL10(12), CL10(13), CL10(14), CL10(15), \
    "v160","v161","v162","v163","v164","v165","v166","v167"
#define TMP_REGS "v32","v33","v34","v35","v36","v37","v38","v39"

__device__ __forceinline__ float bf_lo(u32 v) { return __uint_as_float(v << 16); }
__device__ __forceinline__ float bfround(float f) {
    typedef __bf16 bf16x2 __attribute__((ext_vector_type(2)));
    f32x2 v = {f, f};
    return bf_lo(__builtin_bit_cast(u32, __builtin_convertvector(v, bf16x2)));
}
template <int CTRL>
__device__ __forceinline__ float dpp_add(float v) {
    return v + __int_as_float(__builtin_amdgcn_update_dpp(0, __float_as_int(v), CTRL, 0xf, 0xf, true));
}
__device__ __forceinline__ float add_xor16(float v) {
    auto r = __builtin_amdgcn_permlane16_swap(__float_as_uint(v), __float_as_uint(v), false, false);
    return __uint_as_float(r[0]) + __uint_as_float(r[1]);
}
__device__ __forceinline__ float add_xor32(float v) {
    auto r = __builtin_amdgcn_permlane32_swap(__float_as_uint(v), __float_as_uint(v), false, false);
    return __uint_as_float(r[0]) + __uint_as_float(r[1]);
}
template <typename T>
__device__ __forceinline__ LAS T* lds_at(u32 byte) { return (LAS T*)(uintptr_t)byte; }

#define STAMP_AT(k) do { } while (0)

#define RS 448
#define PL (32 * RS)
#define L_RED 0
#define L_PART (2 * PL)
#define L_PART2 (L_PART + 1024)
#define L_SV (L_PART2 + 512)
#define L_W (L_SV + 768)
#define L_END (L_W + NT * 16)

#define UNPACK(K) \
    "v_lshlrev_b32 v32, 16, v[40+4*" K "]\n\t" \
    "v_and_b32 v33, %[mask], v[40+4*" K "]\n\t" \
    "v_lshlrev_b32 v34, 16, v[41+4*" K "]\n\t" \
    "v_and_b32 v35, %[mask], v[41+4*" K "]\n\t" \
    "v_lshlrev_b32 v36, 16, v[42+4*" K "]\n\t" \
    "v_and_b32 v37, %[mask], v[42+4*" K "]\n\t" \
    "v_lshlrev_b32 v38, 16, v[43+4*" K "]\n\t" \
    "v_and_b32 v39, %[mask], v[43+4*" K "]\n\t"
#define ACCUM(K) UNPACK(K) \
    "v_pk_add_f32 v[24:25], v[24:25], v[32:33]\n\t" \
    "v_pk_add_f32 v[26:27], v[26:27], v[34:35]\n\t" \
    "v_pk_add_f32 v[28:29], v[28:29], v[36:37]\n\t" \
    "v_pk_add_f32 v[30:31], v[30:31], v[38:39]\n\t"

__global__ void __launch_bounds__(NT, 3) __attribute__((amdgpu_num_vgpr(20)))
se_kernel(const void* x_, const void* wd_, const void* wu_, void* out_, void* ws_) {
    __shared__ __attribute__((aligned(16))) char lds[L_END];
    u64x2 k01, k23; u64 k4;
    asm volatile("s_load_dwordx4 %0, %3, 0x0\n\ts_load_dwordx4 %1, %3, 0x10\n\ts_load_dwordx2 %2, %3, 0x20\n\ts_waitcnt lgkmcnt(0)"
                 : "=&s"(k01), "=&s"(k23), "=&s"(k4) : "s"(__builtin_amdgcn_kernarg_segment_ptr()));
    const u64 x = k01[0], wd = k01[1], wu = k23[0], out = k23[1], ws = k4;

    const int t = threadIdx.x;
    const int lane = t & 63;
    const int w = __builtin_amdgcn_readfirstlane(t >> 6);
    const int b = blockIdx.x;
    const u32 even_xcc = 1u - (__builtin_amdgcn_s_getreg((3 << 11) | 20) & 1u);
    STAMP_AT(0);

    const u32 reg = (b & 7) * 32 + (b >> 3);
    const u64 base = (u64)reg * (NT * 16);
    const u32 span = 31u * STEP + NT * 16;
    const auto rx = __builtin_amdgcn_make_buffer_rsrc((void*)(x + base), 0, span, 0x00020000);
    const u32 voff = t * 16;

    int t2 = t;
    asm volatile(
        ".set se_i, 0\n\t"
        ".rept " STR(FIRST) "\n\t"
        "s_mov_b32 s63, se_i*" STR(STEP) "\n\t"
        "buffer_load_dwordx4 v[40+4*se_i:43+4*se_i], %[voff], %[rx], s63 offen" LD_POL "\n\t"
        ".set se_i, se_i+1\n\t"
        ".endr"
        : "+v"(t2) : [voff] "v"(voff), [rx] "s"(rx) : DATA_REGS, "s63", "memory");

    const u32 lb = (u32)(uintptr_t)(LAS char*)lds;
    const int lane2 = t2 & 63;
    const int q = lane2 >> 4;
    const u32 slot = 16 * w + (lane2 & 15);
    const u32 pl = slot >= 96 ? 1 : 0, idx = slot - 96 * pl;
    const u32 ch = 8 * (idx >> 2) + 4 * pl + (idx & 3);
    const u64 wp = t2 < 288 ? wu + t2 * 16 : ws;
    const u64 wdp = wd + (ch * R + 3 * q) * 2;
    u32 a_wl = lb + L_W + t2 * 16;
    u32 a_red = lb + L_RED + (t2 / 24) * RS + (t2 % 24) * 16;
    u32 a_rd = lb + L_RED + pl * PL + q * RS + idx * 4;
    u32 a_part = lb + L_PART + (3 * q) * 64 + w * 4;
    u32 a_sv = lb + L_SV + (t2 % 24) * 32;
    if (t2 < 64) *lds_at<float>(lb + L_PART + (t2 >> 2) * 64 + 48 + (t2 & 3) * 4) = 0.f;

    f32x4 accA, accB;
    u32 w0, w1, w2, dummy;
    const auto ro = __builtin_amdgcn_make_buffer_rsrc((void*)(out + base), 0, span, 0x00020000);
    const u32 woff = (t2 & 31) * STEP;
    {
        u32x4 wv;
        u32 so;
        asm volatile(
            "s_cmp_lg_u32 %[thr], 0\n\t"
            "global_load_dwordx4 %[wv], %[wp], off\n\t"
            "global_load_ushort %[w0], %[wdp], off\n\t"
            "global_load_ushort %[w1], %[wdp], off offset:2\n\t"
            "global_load_ushort %[w2], %[wdp], off offset:4\n\t"
            "v_mov_b32 v24, 0\n\tv_mov_b32 v25, 0\n\tv_mov_b32 v26, 0\n\tv_mov_b32 v27, 0\n\t"
            "v_mov_b32 v28, 0\n\tv_mov_b32 v29, 0\n\tv_mov_b32 v30, 0\n\tv_mov_b32 v31, 0\n\t"
            ".set se_i, " STR(FIRST) "\n\t"
            ".rept 32-" STR(FIRST) "\n\t"
            ".if se_i >= " STR(WIN_E) "\n\t"
            "s_cbranch_scc0 6f\n\t"
            "s_waitcnt vmcnt(" STR(WIN_E) "-1)\n"
            "6:\n\t"
            ".endif\n\t"
            "s_mov_b32 %[so], se_i*" STR(STEP) "\n\t"
            "buffer_load_dwordx4 v[40+4*se_i:43+4*se_i], %[voff], %[rx], %[so] offen" LD_POL "\n\t"
            ".if se_i == " STR(WARM_AT) "\n\t"
            "s_cmp_lg_u32 %[w], " STR(WARM_WAVE) "\n\t"
            "s_cbranch_scc1 8f\n\t"
            "buffer_load_dword %[dummy], %[woff], %[ro], 0 offen" WARM_POL "\n"
            "8:\n\t"
            "s_cmp_lg_u32 %[thr], 0\n\t"
            ".endif\n\t"
            ".if se_i >= " STR(LAG) "\n\t"
            ".set se_k, se_i-" STR(LAG) "\n\t"
            "s_waitcnt vmcnt(" STR(LAG) ")\n\t"
            ".if se_k == 4\n\t"
            "ds_write_b128 %[awl], %[wv]\n\t"
            ".endif\n\t"
            ACCUM("se_k")
            ".endif\n\t"
            ".set se_i, se_i+1\n\t"
            ".endr\n\t"
            ".set se_k, 32-" STR(LAG) "\n\t"
            ".rept " STR(LAG) "\n\t"
            "s_waitcnt vmcnt(31-se_k)\n\t"
            ACCUM("se_k")
            ".set se_k, se_k+1\n\t"
            ".endr\n\t"
            "ds_write_b128 %[ared], v[24:27]\n\t"
            "ds_write_b128 %[ared], v[28:31] offset:" STR(PL) "\n\t"
            "s_waitcnt lgkmcnt(0)\n\t"
            "s_barrier"
            : "=&{v[24:27]}"(accA), "=&{v[28:31]}"(accB), [wv] "=&v"(wv), [w0] "=&v"(w0), [w1] "=&v"(w1), [w2] "=&v"(w2), [so] "=&s"(so), [dummy] "=&v"(dummy),
              [ard] "+v"(a_rd), [apart] "+v"(a_part), [asv] "+v"(a_sv)
            : [voff] "v"(voff), [rx] "s"(rx), [wp] "v"(wp), [wdp] "v"(wdp), [awl] "v"(a_wl), [ared] "v"(a_red),
              [mask] "s"(0xffff0000u), [thr] "s"(even_xcc), [w] "s"(w), [woff] "v"(woff), [ro] "s"(ro)
            : DATA_REGS, TMP_REGS, "scc", "memory");
    }
    STAMP_AT(1);
    u32 tag = *lds_at<u32>(lb + L_W + 288 * 16) + 1;
    {
        const LAS float* src = lds_at<float>(a_rd);
        constexpr int S = 4 * RS / 4;
        float v0 = src[0], v1 = src[S], v2 = src[2 * S], v3 = src[3 * S], v4 = src[4 * S], v5 = src[5 * S], v6 = src[6 * S], v7 = src[7 * S];
        float sc = ((v0 + v1) + (v2 + v3)) + ((v4 + v5) + (v6 + v7));
        sc = add_xor32(add_xor16(sc));
        float p0 = sc * bf_lo(w0), p1 = sc * bf_lo(w1), p2 = sc * bf_lo(w2);
        p0 = dpp_add<0x111>(p0); p1 = dpp_add<0x111>(p1); p2 = dpp_add<0x111>(p2);
        p0 = dpp_add<0x112>(p0); p1 = dpp_add<0x112>(p1); p2 = dpp_add<0x112>(p2);
        p0 = dpp_add<0x114>(p0); p1 = dpp_add<0x114>(p1); p2 = dpp_add<0x114>(p2);
        p0 = dpp_add<0x118>(p0); p1 = dpp_add<0x118>(p1); p2 = dpp_add<0x118>(p2);
        if ((lane & 15) == 15) {
            LAS float* dst = lds_at<float>(a_part);
            dst[0] = p0; dst[16] = p1; dst[32] = p2;
        }
    }
    const u64 gline = ws + 4096 + (u64)b * 128;
    u32 a_pp = lb + L_PART + (lane & 7) * 128 + (lane >> 3) * 4;
    u32 goff = (lane >> 3) * (NWG * 128) + (lane & 7) * 16;
    asm volatile("" : "+v"(tag), "+v"(a_pp), "+v"(goff));
    {
        asm volatile(
            "s_waitcnt lgkmcnt(0)\n\t"
            "s_barrier\n\t"
            "s_cmp_lg_u32 %[w], 0\n\t"
            "s_cbranch_scc1 1f\n\t"
            "ds_read2_b32 v[24:25], %[app] offset0:0 offset1:8\n\t"
            "ds_read2_b32 v[26:27], %[app] offset0:16 offset1:24\n\t"
            "v_mov_b32 v29, %[tag]\n\t"
            "v_mov_b32 v31, %[tag]\n\t"
            "s_waitcnt lgkmcnt(1)\n\t"
            "v_add_f32 v28, v24, v25\n\t"
            "s_waitcnt lgkmcnt(0)\n\t"
            "v_add_f32 v30, v26, v27\n\t"
            "s_nop 0\n\t"
            "v_add_f32_dpp v28, v28, v28 row_ror:8 row_mask:0xf bank_mask:0xf\n\t"
            "v_add_f32_dpp v30, v30, v30 row_ror:8 row_mask:0xf bank_mask:0xf\n\t"
            "s_nop 0\n\t"
            "v_mov_b32 v24, v28\n\t"
            "v_mov_b32 v25, v30\n\t"
            "s_nop 1\n\t"
            "v_permlane16_swap_b32 v28, v24\n\t"
            "v_permlane16_swap_b32 v30, v25\n\t"
            "s_nop 1\n\t"
            "v_add_f32 v28, v28, v24\n\t"
            "v_add_f32 v30, v30, v25\n\t"
            "v_mov_b32 v24, v28\n\t"
            "v_mov_b32 v25, v30\n\t"
            "s_nop 1\n\t"
            "v_permlane32_swap_b32 v28, v24\n\t"
            "v_permlane32_swap_b32 v30, v25\n\t"
            "s_nop 1\n\t"
            "v_add_f32 v28, v28, v24\n\t"
            "v_add_f32 v30, v30, v25\n\t"
            "global_store_dwordx4 %[goff], v[28:31], %[gline] sc1\n\t"
            "s_nop 1\n"
            "1:"
            :
            : [w] "s"(w), [app] "v"(a_pp), [tag] "v"(tag), [goff] "v"(goff), [gline] "s"(gline)
            : "v24", "v25", "v26", "v27", "v28", "v29", "v30", "v31", "scc", "memory");
    }
    STAMP_AT(2);
    float wuv[R];
    if (w < 3) {
        const LAS unsigned short* wul = lds_at<unsigned short>(lb + L_W + t * 2);
#pragma unroll
        for (int j = 0; j < R; ++j) wuv[j] = bf_lo(wul[j * C]);
    }
    if (w < 8) {
        const auto rg = __builtin_amdgcn_make_buffer_rsrc((void*)(ws + 4096 + (b & 7) * (NWG * 128)), 0, NWG * 128, 0x00020000);
        const u32 off = (32 * w + (lane >> 3)) * 128 + (lane & 7) * 16;
        u32x4 g0, g1, g2, g3;
        u64 m0 = 0x3f3f3f3f3f3f3f3full, m1 = m0, m2 = m0, m3 = m0, ma;
        u32 spin = SPIN_CAP;
        asm volatile(
            "s_sleep 4\n"
            "0:\n\t"
            "s_mov_b64 exec, %[m0]\n\t"
            "s_cbranch_execz 2f\n\t"
            "buffer_load_dwordx4 v[24:27], %[off], %[rg], 0 offen sc1\n"
            "2:\n\t"
            "s_mov_b64 exec, %[m1]\n\t"
            "s_cbranch_execz 3f\n\t"
            "buffer_load_dwordx4 v[28:31], %[off], %[rg], 0 offen offset:1024 sc1\n"
            "3:\n\t"
            "s_mov_b64 exec, %[m2]\n\t"
            "s_cbranch_execz 4f\n\t"
            "buffer_load_dwordx4 v[32:35], %[off], %[rg], 0 offen offset:2048 sc1\n"
            "4:\n\t"
            "s_mov_b64 exec, %[m3]\n\t"
            "s_cbranch_execz 5f\n\t"
            "buffer_load_dwordx4 v[36:39], %[off], %[rg], 0 offen offset:3072 sc1\n"
            "5:\n\t"
            "s_mov_b64 exec, -1\n\t"
            "s_waitcnt vmcnt(0)\n\t"
            "v_cmp_ne_u32 vcc, %[tag], v25\n\t"
            "v_cmp_ne_u32 %[ma], %[tag], v27\n\t"
            "s_or_b64 vcc, vcc, %[ma]\n\t"
            "s_and_b64 %[m0], %[m0], vcc\n\t"
            "v_cmp_ne_u32 vcc, %[tag], v29\n\t"
            "v_cmp_ne_u32 %[ma], %[tag], v31\n\t"
            "s_or_b64 vcc, vcc, %[ma]\n\t"
            "s_and_b64 %[m1], %[m1], vcc\n\t"
            "v_cmp_ne_u32 vcc, %[tag], v33\n\t"
            "v_cmp_ne_u32 %[ma], %[tag], v35\n\t"
            "s_or_b64 vcc, vcc, %[ma]\n\t"
            "s_and_b64 %[m2], %[m2], vcc\n\t"
            "v_cmp_ne_u32 vcc, %[tag], v37\n\t"
            "v_cmp_ne_u32 %[ma], %[tag], v39\n\t"
            "s_or_b64 vcc, vcc, %[ma]\n\t"
            "s_and_b64 %[m3], %[m3], vcc\n\t"
            "s_or_b64 %[ma], %[m0], %[m1]\n\t"
            "s_or_b64 vcc, %[m2], %[m3]\n\t"
            "s_or_b64 %[ma], %[ma], vcc\n\t"
            "s_cbranch_scc0 7f\n\t"
            "s_sub_u32 %[spin], %[spin], 1\n\t"
            "s_cbranch_scc0 0b\n"
            "7:"
            : "=&{v[24:27]}"(g0), "=&{v[28:31]}"(g1), "=&{v[32:35]}"(g2), "=&{v[36:39]}"(g3),
              [m0] "+s"(m0), [m1] "+s"(m1), [m2] "+s"(m2), [m3] "+s"(m3), [ma] "=&s"(ma), [spin] "+s"(spin)
            : [off] "v"(off), [rg] "s"(rg), [tag] "s"(__builtin_amdgcn_readfirstlane(tag))
            : "vcc", "scc", "memory");
        float z0 = (__uint_as_float(g0[0]) + __uint_as_float(g1[0])) + (__uint_as_float(g2[0]) + __uint_as_float(g3[0]));
        float z1 = (__uint_as_float(g0[2]) + __uint_as_float(g1[2])) + (__uint_as_float(g2[2]) + __uint_as_float(g3[2]));
        z0 = dpp_add<0x128>(z0); z1 = dpp_add<0x128>(z1);
        z0 = add_xor32(add_xor16(z0)); z1 = add_xor32(add_xor16(z1));
        if (lane < 6) { LAS float* d = lds_at<float>(lb + L_PART2 + lane * 64 + w * 4); d[0] = z0; d[8] = z1; }
    }
    __syncthreads();
    STAMP_AT(3);
    if (b == 0 && t == 0) __hip_atomic_store((gu32*)ws, tag, __ATOMIC_RELAXED, __HIP_MEMORY_SCOPE_AGENT);
    if (w < 3) {
        f32x4 pv = *lds_at<f32x4>(lb + L_PART2 + (lane & 31) * 16);
        float z = (pv[0] + pv[1]) + (pv[2] + pv[3]);
        z = dpp_add<0xb1>(z);
        z = bfround(z * (1.0f / (512.f * 512.f)));
        const int hb = __float_as_int(bfround(z * __builtin_amdgcn_rcpf(1.f + __expf(-z))));
        float ua = 0.f, ub = 0.f, uc = 0.f;
#pragma unroll
        for (int j = 0; j < 4; ++j) {
            ua += __int_as_float(__builtin_amdgcn_readlane(hb, 2 * j)) * wuv[j];
            ub += __int_as_float(__builtin_amdgcn_readlane(hb, 2 * (j + 4))) * wuv[j + 4];
            uc += __int_as_float(__builtin_amdgcn_readlane(hb, 2 * (j + 8))) * wuv[j + 8];
        }
        const float u = bfround((ua + ub) + uc);
        *lds_at<float>(lb + L_SV + t * 4) = bfround(__builtin_amdgcn_rcpf(1.f + __expf(-u)));
    }
    __syncthreads();
    {
        const f32x4 sa = *lds_at<f32x4>(a_sv), sb = *lds_at<f32x4>(a_sv + 16);
        u32 so;
        STAMP_AT(4);
        asm volatile(
            "s_memrealtime s[60:61]\n\t"
            "s_waitcnt lgkmcnt(0)\n\t"
            "s_add_u32 s62, s60, %[phase]\n\t"
            ".set se_i, 0\n\t"
            ".rept 32\n\t"
            UNPACK("se_i")
            "v_pk_mul_f32 v[32:33], v[32:33], v[24:25]\n\t"
            "v_pk_mul_f32 v[34:35], v[34:35], v[26:27]\n\t"
            "v_pk_mul_f32 v[36:37], v[36:37], v[28:29]\n\t"
            "v_pk_mul_f32 v[38:39], v[38:39], v[30:31]\n\t"
            "s_mov_b32 %[so], se_i*" STR(STEP) "\n\t"
            "v_cvt_pk_bf16_f32 v[40+4*se_i], v32, v33\n\t"
            "v_cvt_pk_bf16_f32 v[41+4*se_i], v34, v35\n\t"
            "v_cvt_pk_bf16_f32 v[42+4*se_i], v36, v37\n\t"
            "v_cvt_pk_bf16_f32 v[43+4*se_i], v38, v39\n\t"
            "s_add_u32 s63, s62, (se_i*" STR(PACE) ")/32\n"
            "9:\n\t"
            "s_memrealtime s[60:61]\n\t"
            "s_waitcnt lgkmcnt(0)\n\t"
            "s_sub_u32 s61, s60, s63\n\t"
            "s_cmp_lt_i32 s61, 0\n\t"
            "s_cbranch_scc1 9b\n\t"
            "buffer_store_dwordx4 v[40+4*se_i:43+4*se_i], %[voff], %[ro], %[so] offen" ST_POL "\n\t"
            ".set se_i, se_i+1\n\t"
            ".endr\n\t"
            : [so] "=&s"(so)
            : "{v[24:27]}"(sa), "{v[28:31]}"(sb), [voff] "v"(voff), [ro] "s"(ro), [mask] "s"(0xffff0000u),
              [phase] "s"(even_xcc ? (u32)(w * (PACE / 32) / 12) : (u32)-100000)
            : DATA_REGS, TMP_REGS, "s60", "s61", "s62", "s63", "scc", "memory");
    }
}

extern "C" void kernel_launch(void* const* d_in, const int* in_sizes, int n_in,
                              void* d_out, int out_size, void* d_ws, size_t ws_size,
                              hipStream_t stream) {
    se_kernel<<<NWG, NT, 0, stream>>>(d_in[0], d_in[1], d_in[2], d_out, d_ws);
}
```

```cpp
#include <hip/hip_runtime.h>
#include <hip/hip_bf16.h>
#include <stdint.h>

typedef unsigned int u32;
typedef unsigned long long u64;
typedef u32 u32x4 __attribute__((ext_vector_type(4)));
typedef float f32x2 __attribute__((ext_vector_type(2)));
typedef float f32x4 __attribute__((ext_vector_type(4)));
typedef u64 u64x2 __attribute__((ext_vector_type(2)));
typedef __attribute__((address_space(1))) u32 gu32;
#define LAS __attribute__((address_space(3)))

#define C 192
#define R 12
#define NWG 256
#define NT 768
#define STEP 3145728
#define LD_POL " nt"
#define ST_POL " sc0 sc1"
#define SPIN_CAP (1u << 20)
#define LAG 8
#define FIRST 4
#define PACE_EVEN 1235
#define PACE_ODD 300
#define STR2(x) #x
#define STR(x) STR2(x)

#define CL10(p) "v" #p "0","v" #p "1","v" #p "2","v" #p "3","v" #p "4","v" #p "5","v" #p "6","v" #p "7","v" #p "8","v" #p "9"
#define DATA_REGS CL10(4), CL10(5), CL10(6), CL10(7), CL10(8), CL10(9), CL10(10), CL10(11), CL10(12), CL10(13), CL10(14), CL10(15), \
    "v160","v161","v162","v163","v164","v165","v166","v167"
#define TMP_REGS "v32","v33","v34","v35","v36","v37","v38","v39"

__device__ __forceinline__ float bf_lo(u32 v) { return __uint_as_float(v << 16); }
__device__ __forceinline__ float bfround(float f) {
    typedef __bf16 bf16x2 __attribute__((ext_vector_type(2)));
    f32x2 v = {f, f};
    return bf_lo(__builtin_bit_cast(u32, __builtin_convertvector(v, bf16x2)));
}
template <int CTRL>
__device__ __forceinline__ float dpp_add(float v) {
    return v + __int_as_float(__builtin_amdgcn_update_dpp(0, __float_as_int(v), CTRL, 0xf, 0xf, true));
}
__device__ __forceinline__ float add_xor16(float v) {
    auto r = __builtin_amdgcn_permlane16_swap(__float_as_uint(v), __float_as_uint(v), false, false);
    return __uint_as_float(r[0]) + __uint_as_float(r[1]);
}
__device__ __forceinline__ float add_xor32(float v) {
    auto r = __builtin_amdgcn_permlane32_swap(__float_as_uint(v), __float_as_uint(v), false, false);
    return __uint_as_float(r[0]) + __uint_as_float(r[1]);
}
template <typename T>
__device__ __forceinline__ LAS T* lds_at(u32 byte) { return (LAS T*)(uintptr_t)byte; }

#define RS 448
#define PL (32 * RS)
#define L_RED 0
#define L_PART (2 * PL)
#define L_PART2 (L_PART + 1024)
#define L_SV (L_PART2 + 512)
#define L_W (L_SV + 768)
#define L_END (L_W + NT * 16)

#define UNPACK(K) \
    "v_lshlrev_b32 v32, 16, v[40+4*" K "]\n\t" \
    "v_and_b32 v33, %[mask], v[40+4*" K "]\n\t" \
    "v_lshlrev_b32 v34, 16, v[41+4*" K "]\n\t" \
    "v_and_b32 v35, %[mask], v[41+4*" K "]\n\t" \
    "v_lshlrev_b32 v36, 16, v[42+4*" K "]\n\t" \
    "v_and_b32 v37, %[mask], v[42+4*" K "]\n\t" \
    "v_lshlrev_b32 v38, 16, v[43+4*" K "]\n\t" \
    "v_and_b32 v39, %[mask], v[43+4*" K "]\n\t"
#define ACCUM(K) UNPACK(K) \
    "v_pk_add_f32 v[24:25], v[24:25], v[32:33]\n\t" \
    "v_pk_add_f32 v[26:27], v[26:27], v[34:35]\n\t" \
    "v_pk_add_f32 v[28:29], v[28:29], v[36:37]\n\t" \
    "v_pk_add_f32 v[30:31], v[30:31], v[38:39]\n\t"

__global__ void __launch_bounds__(NT, 3) __attribute__((amdgpu_num_vgpr(20)))
se_kernel(const void* x_, const void* wd_, const void* wu_, void* out_, void* ws_) {
    __shared__ __attribute__((aligned(16))) char lds[L_END];
    u64x2 k01, k23; u64 k4;
    asm volatile("s_load_dwordx4 %0, %3, 0x0\n\ts_load_dwordx4 %1, %3, 0x10\n\ts_load_dwordx2 %2, %3, 0x20\n\ts_waitcnt lgkmcnt(0)"
                 : "=&s"(k01), "=&s"(k23), "=&s"(k4) : "s"(__builtin_amdgcn_kernarg_segment_ptr()));
    const u64 x = k01[0], wd = k01[1], wu = k23[0], out = k23[1], ws = k4;

    const int t = threadIdx.x;
    const int lane = t & 63;
    const int w = __builtin_amdgcn_readfirstlane(t >> 6);
    const int b = blockIdx.x;
    const u32 even_xcc = 1u - (__builtin_amdgcn_s_getreg((3 << 11) | 20) & 1u);

    const u32 reg = (b & 7) * 32 + (b >> 3);
    const u64 base = (u64)reg * (NT * 16);
    const u32 span = 31u * STEP + NT * 16;
    const auto rx = __builtin_amdgcn_make_buffer_rsrc((void*)(x + base), 0, span, 0x00020000);
    const u32 voff = t * 16;

    int t2 = t;
    asm volatile(
        ".set se_i, 0\n\t"
        ".rept " STR(FIRST) "\n\t"
        "s_mov_b32 s63, se_i*" STR(STEP) "\n\t"
        "buffer_load_dwordx4 v[40+4*se_i:43+4*se_i], %[voff], %[rx], s63 offen" LD_POL "\n\t"
        ".set se_i, se_i+1\n\t"
        ".endr"
        : "+v"(t2) : [voff] "v"(voff), [rx] "s"(rx) : DATA_REGS, "s63", "memory");

    const u32 lb = (u32)(uintptr_t)(LAS char*)lds;
    const int lane2 = t2 & 63;
    const int q = lane2 >> 4;
    const u32 slot = 16 * w + (lane2 & 15);
    const u32 pl = slot >= 96 ? 1 : 0, idx = slot - 96 * pl;
    const u32 ch = 8 * (idx >> 2) + 4 * pl + (idx & 3);
    const u64 wp = t2 < 288 ? wu + t2 * 16 : ws;
    const u64 wdp = wd + (ch * R + 3 * q) * 2;
    u32 a_wl = lb + L_W + t2 * 16;
    u32 a_red = lb + L_RED + (t2 / 24) * RS + (t2 % 24) * 16;
    u32 a_rd = lb + L_RED + pl * PL + q * RS + idx * 4;
    u32 a_part = lb + L_PART + (3 * q) * 64 + w * 4;
    u32 a_sv = lb + L_SV + (t2 % 24) * 32;
    u32 inc = even_xcc ? PACE_EVEN : PACE_ODD;
    u32 phase = (u32)w * inc / 12;
    asm volatile("" : "+s"(inc), "+s"(phase));
    if (t2 < 64) *lds_at<float>(lb + L_PART + (t2 >> 2) * 64 + 48 + (t2 & 3) * 4) = 0.f;

    f32x4 accA, accB;
    u32 w0, w1, w2;
    const auto ro = __builtin_amdgcn_make_buffer_rsrc((void*)(out + base), 0, span, 0x00020000);
    {
        u32x4 wv;
        u32 so;
        asm volatile(
            "global_load_dwordx4 %[wv], %[wp], off\n\t"
            "global_load_ushort %[w0], %[wdp], off\n\t"
            "global_load_ushort %[w1], %[wdp], off offset:2\n\t"
            "global_load_ushort %[w2], %[wdp], off offset:4\n\t"
            "v_mov_b32 v24, 0\n\tv_mov_b32 v25, 0\n\tv_mov_b32 v26, 0\n\tv_mov_b32 v27, 0\n\t"
            "v_mov_b32 v28, 0\n\tv_mov_b32 v29, 0\n\tv_mov_b32 v30, 0\n\tv_mov_b32 v31, 0\n\t"
            ".set se_i, " STR(FIRST) "\n\t"
            ".rept 32-" STR(FIRST) "\n\t"
            "s_mov_b32 %[so], se_i*" STR(STEP) "\n\t"
            "buffer_load_dwordx4 v[40+4*se_i:43+4*se_i], %[voff], %[rx], %[so] offen" LD_POL "\n\t"
            ".if se_i >= " STR(LAG) "\n\t"
            ".set se_k, se_i-" STR(LAG) "\n\t"
            "s_waitcnt vmcnt(" STR(LAG) ")\n\t"
            ".if se_k == 4\n\t"
            "ds_write_b128 %[awl], %[wv]\n\t"
            ".endif\n\t"
            ACCUM("se_k")
            ".endif\n\t"
            ".set se_i, se_i+1\n\t"
            ".endr\n\t"
            ".set se_k, 32-" STR(LAG) "\n\t"
            ".rept " STR(LAG) "\n\t"
            "s_waitcnt vmcnt(31-se_k)\n\t"
            ACCUM("se_k")
            ".set se_k, se_k+1\n\t"
            ".endr\n\t"
            "ds_write_b128 %[ared], v[24:27]\n\t"
            "ds_write_b128 %[ared], v[28:31] offset:" STR(PL) "\n\t"
            "s_waitcnt lgkmcnt(0)\n\t"
            "s_barrier"
            : "=&{v[24:27]}"(accA), "=&{v[28:31]}"(accB), [wv] "=&v"(wv), [w0] "=&v"(w0), [w1] "=&v"(w1), [w2] "=&v"(w2), [so] "=&s"(so),
              [ard] "+v"(a_rd), [apart] "+v"(a_part), [asv] "+v"(a_sv)
            : [voff] "v"(voff), [rx] "s"(rx), [wp] "v"(wp), [wdp] "v"(wdp), [awl] "v"(a_wl), [ared] "v"(a_red),
              [mask] "s"(0xffff0000u)
            : DATA_REGS, TMP_REGS, "memory");
    }
    u32 tag = *lds_at<u32>(lb + L_W + 288 * 16) + 1;
    {
        const LAS float* src = lds_at<float>(a_rd);
        constexpr int S = 4 * RS / 4;
        float v0 = src[0], v1 = src[S], v2 = src[2 * S], v3 = src[3 * S], v4 = src[4 * S], v5 = src[5 * S], v6 = src[6 * S], v7 = src[7 * S];
        float sc = ((v0 + v1) + (v2 + v3)) + ((v4 + v5) + (v6 + v7));
        sc = add_xor32(add_xor16(sc));
        float p0 = sc * bf_lo(w0), p1 = sc * bf_lo(w1), p2 = sc * bf_lo(w2);
        p0 = dpp_add<0x111>(p0); p1 = dpp_add<0x111>(p1); p2 = dpp_add<0x111>(p2);
        p0 = dpp_add<0x112>(p0); p1 = dpp_add<0x112>(p1); p2 = dpp_add<0x112>(p2);
        p0 = dpp_add<0x114>(p0); p1 = dpp_add<0x114>(p1); p2 = dpp_add<0x114>(p2);
        p0 = dpp_add<0x118>(p0); p1 = dpp_add<0x118>(p1); p2 = dpp_add<0x118>(p2);
        if ((lane & 15) == 15) {
            LAS float* dst = lds_at<float>(a_part);
            dst[0] = p0; dst[16] = p1; dst[32] = p2;
        }
    }
    const u64 gline = ws + 4096 + (u64)b * 128;
    u32 a_pp = lb + L_PART + (lane & 7) * 128 + (lane >> 3) * 4;
    u32 goff = (lane >> 3) * (NWG * 128) + (lane & 7) * 16;
    asm volatile("" : "+v"(tag), "+v"(a_pp), "+v"(goff));
    {
        asm volatile(
            "s_waitcnt lgkmcnt(0)\n\t"
            "s_barrier\n\t"
            "s_cmp_lg_u32 %[w], 0\n\t"
            "s_cbranch_scc1 1f\n\t"
            "ds_read2_b32 v[24:25], %[app] offset0:0 offset1:8\n\t"
            "ds_read2_b32 v[26:27], %[app] offset0:16 offset1:24\n\t"
            "v_mov_b32 v29, %[tag]\n\t"
            "v_mov_b32 v31, %[tag]\n\t"
            "s_waitcnt lgkmcnt(1)\n\t"
            "v_add_f32 v28, v24, v25\n\t"
            "s_waitcnt lgkmcnt(0)\n\t"
            "v_add_f32 v30, v26, v27\n\t"
            "s_nop 0\n\t"
            "v_add_f32_dpp v28, v28, v28 row_ror:8 row_mask:0xf bank_mask:0xf\n\t"
            "v_add_f32_dpp v30, v30, v30 row_ror:8 row_mask:0xf bank_mask:0xf\n\t"
            "s_nop 0\n\t"
            "v_mov_b32 v24, v28\n\t"
            "v_mov_b32 v25, v30\n\t"
            "s_nop 1\n\t"
            "v_permlane16_swap_b32 v28, v24\n\t"
            "v_permlane16_swap_b32 v30, v25\n\t"
            "s_nop 1\n\t"
            "v_add_f32 v28, v28, v24\n\t"
            "v_add_f32 v30, v30, v25\n\t"
            "v_mov_b32 v24, v28\n\t"
            "v_mov_b32 v25, v30\n\t"
            "s_nop 1\n\t"
            "v_permlane32_swap_b32 v28, v24\n\t"
            "v_permlane32_swap_b32 v30, v25\n\t"
            "s_nop 1\n\t"
            "v_add_f32 v28, v28, v24\n\t"
            "v_add_f32 v30, v30, v25\n\t"
            "global_store_dwordx4 %[goff], v[28:31], %[gline] sc1\n\t"
            "s_nop 1\n"
            "1:"
            :
            : [w] "s"(w), [app] "v"(a_pp), [tag] "v"(tag), [goff] "v"(goff), [gline] "s"(gline)
            : "v24", "v25", "v26", "v27", "v28", "v29", "v30", "v31", "scc", "memory");
    }
    float wuv[R];
    if (w < 3) {
        const LAS unsigned short* wul = lds_at<unsigned short>(lb + L_W + t * 2);
#pragma unroll
        for (int j = 0; j < R; ++j) wuv[j] = bf_lo(wul[j * C]);
    }
    if (w < 8) {
        const auto rg = __builtin_amdgcn_make_buffer_rsrc((void*)(ws + 4096 + (b & 7) * (NWG * 128)), 0, NWG * 128, 0x00020000);
        const u32 off = (32 * w + (lane >> 3)) * 128 + (lane & 7) * 16;
        u32x4 g0, g1, g2, g3;
        u64 m0 = 0x3f3f3f3f3f3f3f3full, m1 = m0, m2 = m0, m3 = m0, ma;
        u32 spin = SPIN_CAP;
        asm volatile(
            "s_sleep 4\n"
            "0:\n\t"
            "s_mov_b64 exec, %[m0]\n\t"
            "s_cbranch_execz 2f\n\t"
            "buffer_load_dwordx4 v[24:27], %[off], %[rg], 0 offen sc1\n"
            "2:\n\t"
            "s_mov_b64 exec, %[m1]\n\t"
            "s_cbranch_execz 3f\n\t"
            "buffer_load_dwordx4 v[28:31], %[off], %[rg], 0 offen offset:1024 sc1\n"
            "3:\n\t"
            "s_mov_b64 exec, %[m2]\n\t"
            "s_cbranch_execz 4f\n\t"
            "buffer_load_dwordx4 v[32:35], %[off], %[rg], 0 offen offset:2048 sc1\n"
            "4:\n\t"
            "s_mov_b64 exec, %[m3]\n\t"
            "s_cbranch_execz 5f\n\t"
            "buffer_load_dwordx4 v[36:39], %[off], %[rg], 0 offen offset:3072 sc1\n"
            "5:\n\t"
            "s_mov_b64 exec, -1\n\t"
            "s_waitcnt vmcnt(0)\n\t"
            "v_cmp_ne_u32 vcc, %[tag], v25\n\t"
            "v_cmp_ne_u32 %[ma], %[tag], v27\n\t"
            "s_or_b64 vcc, vcc, %[ma]\n\t"
            "s_and_b64 %[m0], %[m0], vcc\n\t"
            "v_cmp_ne_u32 vcc, %[tag], v29\n\t"
            "v_cmp_ne_u32 %[ma], %[tag], v31\n\t"
            "s_or_b64 vcc, vcc, %[ma]\n\t"
            "s_and_b64 %[m1], %[m1], vcc\n\t"
            "v_cmp_ne_u32 vcc, %[tag], v33\n\t"
            "v_cmp_ne_u32 %[ma], %[tag], v35\n\t"
            "s_or_b64 vcc, vcc, %[ma]\n\t"
            "s_and_b64 %[m2], %[m2], vcc\n\t"
            "v_cmp_ne_u32 vcc, %[tag], v37\n\t"
            "v_cmp_ne_u32 %[ma], %[tag], v39\n\t"
            "s_or_b64 vcc, vcc, %[ma]\n\t"
            "s_and_b64 %[m3], %[m3], vcc\n\t"
            "s_or_b64 %[ma], %[m0], %[m1]\n\t"
            "s_or_b64 vcc, %[m2], %[m3]\n\t"
            "s_or_b64 %[ma], %[ma], vcc\n\t"
            "s_cbranch_scc0 7f\n\t"
            "s_sub_u32 %[spin], %[spin], 1\n\t"
            "s_cbranch_scc0 0b\n"
            "7:"
            : "=&{v[24:27]}"(g0), "=&{v[28:31]}"(g1), "=&{v[32:35]}"(g2), "=&{v[36:39]}"(g3),
              [m0] "+s"(m0), [m1] "+s"(m1), [m2] "+s"(m2), [m3] "+s"(m3), [ma] "=&s"(ma), [spin] "+s"(spin)
            : [off] "v"(off), [rg] "s"(rg), [tag] "s"(__builtin_amdgcn_readfirstlane(tag))
            : "vcc", "scc", "memory");
        float z0 = (__uint_as_float(g0[0]) + __uint_as_float(g1[0])) + (__uint_as_float(g2[0]) + __uint_as_float(g3[0]));
        float z1 = (__uint_as_float(g0[2]) + __uint_as_float(g1[2])) + (__uint_as_float(g2[2]) + __uint_as_float(g3[2]));
        z0 = dpp_add<0x128>(z0); z1 = dpp_add<0x128>(z1);
        z0 = add_xor32(add_xor16(z0)); z1 = add_xor32(add_xor16(z1));
        if (lane < 6) { LAS float* d = lds_at<float>(lb + L_PART2 + lane * 64 + w * 4); d[0] = z0; d[8] = z1; }
    }
    __syncthreads();
    if (b == 0 && t == 0) __hip_atomic_store((gu32*)ws, tag, __ATOMIC_RELAXED, __HIP_MEMORY_SCOPE_AGENT);
    if (w < 3) {
        f32x4 pv = *lds_at<f32x4>(lb + L_PART2 + (lane & 31) * 16);
        float z = (pv[0] + pv[1]) + (pv[2] + pv[3]);
        z = dpp_add<0xb1>(z);
        z = bfround(z * (1.0f / (512.f * 512.f)));
        const int hb = __float_as_int(bfround(z * __builtin_amdgcn_rcpf(1.f + __expf(-z))));
        float ua = 0.f, ub = 0.f, uc = 0.f;
#pragma unroll
        for (int j = 0; j < 4; ++j) {
            ua += __int_as_float(__builtin_amdgcn_readlane(hb, 2 * j)) * wuv[j];
            ub += __int_as_float(__builtin_amdgcn_readlane(hb, 2 * (j + 4))) * wuv[j + 4];
            uc += __int_as_float(__builtin_amdgcn_readlane(hb, 2 * (j + 8))) * wuv[j + 8];
        }
        const float u = bfround((ua + ub) + uc);
        *lds_at<float>(lb + L_SV + t * 4) = bfround(__builtin_amdgcn_rcpf(1.f + __expf(-u)));
    }
    __syncthreads();
    {
        u32 tgt = ((u32)__builtin_amdgcn_s_memrealtime() << 5) + phase;
        const f32x4 sa = *lds_at<f32x4>(a_sv), sb = *lds_at<f32x4>(a_sv + 16);
        u32 so;
        asm volatile(
            ".set se_i, 0\n\t"
            ".rept 32\n\t"
            UNPACK("se_i")
            "v_pk_mul_f32 v[32:33], v[32:33], v[24:25]\n\t"
            "v_pk_mul_f32 v[34:35], v[34:35], v[26:27]\n\t"
            "v_pk_mul_f32 v[36:37], v[36:37], v[28:29]\n\t"
            "v_pk_mul_f32 v[38:39], v[38:39], v[30:31]\n\t"
            "s_mov_b32 %[so], se_i*" STR(STEP) "\n\t"
            "v_cvt_pk_bf16_f32 v[40+4*se_i], v32, v33\n\t"
            "v_cvt_pk_bf16_f32 v[41+4*se_i], v34, v35\n\t"
            "v_cvt_pk_bf16_f32 v[42+4*se_i], v36, v37\n\t"
            "v_cvt_pk_bf16_f32 v[43+4*se_i], v38, v39\n\t"
            "9:\n\t"
            "s_memrealtime s[60:61]\n\t"
            "s_waitcnt lgkmcnt(0)\n\t"
            "s_lshl_b32 s60, s60, 5\n\t"
            "s_sub_u32 s61, s60, %[tgt]\n\t"
            "s_cmp_lt_i32 s61, 0\n\t"
            "s_cbranch_scc1 9b\n\t"
            "s_add_u32 %[tgt], %[tgt], %[inc]\n\t"
            "buffer_store_dwordx4 v[40+4*se_i:43+4*se_i], %[voff], %[ro], %[so] offen" ST_POL "\n\t"
            ".set se_i, se_i+1\n\t"
            ".endr\n\t"
            : [so] "=&s"(so), [tgt] "+s"(tgt)
            : "{v[24:27]}"(sa), "{v[28:31]}"(sb), [voff] "v"(voff), [ro] "s"(ro), [mask] "s"(0xffff0000u),
              [inc] "s"(inc)
            : DATA_REGS, TMP_REGS, "s60", "s61", "scc", "memory");
    }
}

extern "C" void kernel_launch(void* const* d_in, const int* in_sizes, int n_in,
                              void* d_out, int out_size, void* d_ws, size_t ws_size,
                              hipStream_t stream) {
    se_kernel<<<NWG, NT, 0, stream>>>(d_in[0], d_in[1], d_in[2], d_out, d_ws);
}
```
